```python
import torch

def _softmax(x, axis=-1):
    return torch.softmax(x, dim=axis)

def _gelu(x):
    return torch.nn.functional.gelu(x, approximate='tanh')

def _silu(x):
    return torch.nn.functional.silu(x)

def _sigmoid(x):
    return torch.sigmoid(x)

def _layernorm(x, axis=-1, eps=1e-05):
    m = x.mean(dim=axis, keepdim=True)
    v = x.var(dim=axis, keepdim=True, unbiased=False)
    return (x - m) / torch.sqrt(v + eps)

def _scatter_add(tgt, idx, src, axis=0):
    return tgt.clone().index_add_(axis, idx, src)

def _segment_sum(src, seg, n):
    out = torch.zeros((n,) + src.shape[1:], dtype=src.dtype)
    return out.index_add_(0, seg, src)

def _pad(x, pw):
    flat = tuple((v for pair in reversed(pw) for v in pair))
    return torch.nn.functional.pad(x, flat)
kernel_name = 'sequential_preLN__memory_aug_recurrent_mem_tr__glu_gated_geglu__prompt'

def setup_inputs():

    _w = lambda a: (lambda t: t.to(torch.bfloat16) if t.is_floating_point() else t)(torch.as_tensor(a))

    def _A_setup():
        B, S, Dm = (1, 2048, 512)
        H, Dh = (32, 128)
        M, seg = (8, 32)
        seg = min(seg, S)
        S = S // seg * seg
        return {'x': _w((lambda *s: torch.randn(*s, dtype=torch.bfloat16) * 0.05)(B, S, Dm)), 'mem0': _w((lambda *s: torch.randn(*s, dtype=torch.bfloat16) * 0.2)(B, M, Dm)), 'Wq': _w((lambda *s: torch.randn(*s, dtype=torch.bfloat16) * 1.6)(Dm, H, Dh) * Dm ** (-0.5)), 'Wk': _w((lambda *s: torch.randn(*s, dtype=torch.bfloat16) * 1.6)(Dm, H, Dh) * Dm ** (-0.5)), 'Wv': _w((lambda *s: torch.randn(*s, dtype=torch.bfloat16) * 0.2)(Dm, H, Dh) * Dm ** (-0.5)), 'Wo': _w((lambda *s: torch.randn(*s, dtype=torch.bfloat16) * 0.2)(H, Dh, Dm) * (H * Dh) ** (-0.5)), 'seg_len': seg}
    _A_in = _A_setup()

    def _B_setup():
        B, S, Dm, Df = (1, 2048, 512, 1376)
        return {'x': _w((lambda *s: torch.randn(*s, dtype=torch.bfloat16) * 0.05)(B, S, Dm)), 'w_gate': _w((lambda *s: torch.randn(*s, dtype=torch.bfloat16) * 0.4)(Dm, Df) * Dm ** (-0.5)), 'w_up': _w((lambda *s: torch.randn(*s, dtype=torch.bfloat16) * 0.4)(Dm, Df) * Dm ** (-0.5)), 'w_down': _w((lambda *s: torch.randn(*s, dtype=torch.bfloat16) * 0.4)(Df, Dm) * Df ** (-0.5))}
    _B_in = _B_setup()
    return {'A_x': _A_in['x'], 'A_mem0': _A_in['mem0'], 'A_Wq': _A_in['Wq'], 'A_Wk': _A_in['Wk'], 'A_Wv': _A_in['Wv'], 'A_Wo': _A_in['Wo'], 'A_seg_len': _A_in['seg_len'], 'B_x': _B_in['x'], 'B_w_gate': _B_in['w_gate'], 'B_w_up': _B_in['w_up'], 'B_w_down': _B_in['w_down']}

def reference(A_x, A_mem0, A_Wq, A_Wk, A_Wv, A_Wo, A_seg_len, B_x, B_w_gate, B_w_up, B_w_down):

    def _A_ref(x, mem0, Wq, Wk, Wv, Wo, seg_len):
        B, S, Dm = x.shape
        H, Dh = (Wq.shape[1], Wq.shape[2])
        M = mem0.shape[1]
        scale = Dh ** (-0.5)
        n_seg = S // seg_len
        L = M + seg_len
        idx = torch.arange(L)
        mask = (idx[:, None] < M) | (idx[:, None] >= idx[None, :])
        mem = mem0
        outs = []
        for s in range(n_seg):
            xs = x[:, s * seg_len:(s + 1) * seg_len, :].clone()
            h = torch.concatenate([mem, xs], axis=1)
            q = torch.einsum('btd,dhe->bhte', h, Wq)
            k = torch.einsum('btd,dhe->bhte', h, Wk)
            v = torch.einsum('btd,dhe->bhte', h, Wv)
            scores = torch.einsum('bhqe,bhke->bhqk', q, k) * scale
            scores = torch.where(mask[None, None], scores, -1000000000.0)
            p = _softmax(scores, axis=-1)
            o = torch.einsum('bhqk,bhke->bhqe', p, v)
            o = torch.einsum('bhte,hed->btd', o, Wo)
            mem = o[:, :M, :].clone()
            outs.append(o[:, M:, :])
        return torch.concatenate(outs, axis=1)

    def _B_ref(x, w_gate, w_up, w_down):
        g = torch.einsum('bsd,df->bsf', x, w_gate)
        u = torch.einsum('bsd,df->bsf', x, w_up)
        h = _gelu(g) * u
        return torch.einsum('bsf,fd->bsd', h, w_down)
    x = A_x
    h = x + _A_ref(_layernorm(x, axis=-1), A_mem0, A_Wq, A_Wk, A_Wv, A_Wo, A_seg_len)
    h2 = h + _B_ref(_layernorm(h, axis=-1), B_w_gate, B_w_up, B_w_down)
    return h2
```

```cpp
#include <hip/hip_runtime.h>
#include <hip/hip_bf16.h>
#include <stdint.h>

constexpr int S_ = 2048, DM = 512, NH = 32, DH = 128, HD = 4096, SEG = 32, NSEG = 64, DF = 1376, DFP = 1408;

typedef __attribute__((ext_vector_type(8))) __bf16 bf16x8;
typedef __attribute__((ext_vector_type(16))) __bf16 bf16x16;
typedef __attribute__((ext_vector_type(4))) __bf16 bf16x4;
typedef __attribute__((ext_vector_type(2))) __bf16 bf16x2;
typedef __attribute__((ext_vector_type(4))) short s16x4;
typedef __attribute__((ext_vector_type(8))) short s16x8;
typedef __attribute__((ext_vector_type(4))) float f32x4;
typedef __attribute__((ext_vector_type(4))) unsigned u32x4;
typedef __attribute__((ext_vector_type(2))) unsigned u32x2;
typedef unsigned short u16;
#define LAS __attribute__((address_space(3)))
#define DI __device__ __forceinline__

constexpr size_t WS_XN = 0;
constexpr size_t WS_Q = WS_XN + (size_t)S_ * DM * 2;
constexpr size_t WS_K = WS_Q + (size_t)S_ * HD * 2;
constexpr size_t WS_VT = WS_K + (size_t)S_ * HD * 2;
constexpr size_t WS_KM = WS_VT + (size_t)S_ * HD * 2;
constexpr size_t WS_VM = WS_KM + (size_t)NSEG * NH * 2048;
constexpr size_t WS_O = WS_VM + (size_t)NSEG * NH * 4096;
constexpr size_t WS_H = WS_O + (size_t)S_ * HD * 2;
constexpr size_t WS_HN = WS_H + (size_t)S_ * DM * 4;
constexpr size_t WS_ACT = WS_HN + (size_t)S_ * DM * 2;
constexpr size_t WS_CTL = WS_ACT + (size_t)S_ * DFP * 2;
constexpr size_t WS_EX_MIN = WS_CTL + 65536;
constexpr unsigned EX_PART = 0;
constexpr unsigned EX_MEM = 64u * 32u * 8192u;
constexpr unsigned EX_LOGICAL = EX_MEM + 65u * 8192u;
constexpr unsigned EX_PHYS = 2u * EX_LOGICAL + 16384u;

DI unsigned exphys(unsigned L, unsigned p) {
  unsigned x = ((L >> 13) << 14) | (L & 8191u);
  return x | ((p ^ ((x >> 21) & 1u)) << 13);
}

DI float bf2f(u16 v) { return __uint_as_float(((unsigned)v) << 16); }
DI unsigned pack2(float a, float b) {
  bf16x2 v; v[0] = (__bf16)a; v[1] = (__bf16)b;
  return __builtin_bit_cast(unsigned, v);
}
DI unsigned off_b(unsigned row, unsigned ch) { return 256u * row + 16u * (ch ^ (((row & 3) << 2) | ((row >> 2) & 3))); }
DI s16x4 tr_read(const char* lds_base, unsigned off) {
  return __builtin_amdgcn_ds_read_tr16_b64_v4i16((LAS s16x4*)((LAS char*)lds_base + off));
}
DI s16x4 tr_read_l(LAS char* a) { return __builtin_amdgcn_ds_read_tr16_b64_v4i16((LAS s16x4*)a); }
DI void glds16(const void* g, char* l) {
  __builtin_amdgcn_global_load_lds((const __attribute__((address_space(1))) unsigned*)g, (LAS unsigned*)l, 16, 0, 0);
}
DI void barrier_raw() {
  asm volatile("s_waitcnt lgkmcnt(0)" ::: "memory");
  __builtin_amdgcn_s_barrier();
  asm volatile("" ::: "memory");
}

#define STAMP(k)
struct Params {
  const u16* x; const u16* mem0; const u16* wq; const u16* wk; const u16* wv; const u16* wo;
  const u16* wg; const u16* wu; const u16* wd;
  u16* out; char* ws; char* ex;
};

DI void ln_row(const float (&v)[8], float& mean, float& rstd) {
  float s = 0.f;
#pragma unroll
  for (int j = 0; j < 8; ++j) s += v[j];
#pragma unroll
  for (int m = 1; m < 64; m <<= 1) s += __shfl_xor(s, m);
  mean = s * (1.0f / DM);
  float q = 0.f;
#pragma unroll
  for (int j = 0; j < 8; ++j) { float d = v[j] - mean; q += d * d; }
#pragma unroll
  for (int m = 1; m < 64; m <<= 1) q += __shfl_xor(q, m);
  rstd = 1.0f / sqrtf(q * (1.0f / DM) + 1e-5f);
}

__global__ __launch_bounds__(256) void k0_prologue(Params p) {
  int wave = threadIdx.x >> 6, lane = threadIdx.x & 63;
  int gw = blockIdx.x * 4 + wave;
  u16* xn = (u16*)(p.ws + WS_XN);
  for (int r = gw; r < S_; r += 1024) {
    u32x4 raw = *(const u32x4*)(p.x + (size_t)r * DM + lane * 8);
    float v[8];
#pragma unroll
    for (int j = 0; j < 4; ++j) { v[2 * j] = __uint_as_float(raw[j] << 16); v[2 * j + 1] = __uint_as_float(raw[j] & 0xffff0000u); }
    float mean, rstd; ln_row(v, mean, rstd);
    u32x4 o;
#pragma unroll
    for (int j = 0; j < 4; ++j) o[j] = pack2((v[2 * j] - mean) * rstd, (v[2 * j + 1] - mean) * rstd);
    *(u32x4*)(xn + (size_t)r * DM + lane * 8) = o;
  }
  u32x4 sent = {0xffffffffu, 0xffffffffu, 0xffffffffu, 0xffffffffu};
  for (size_t i = (size_t)blockIdx.x * 256 + threadIdx.x; i < EX_PHYS / 16; i += 65536) ((u32x4*)p.ex)[i] = sent;
  u16* act = (u16*)(p.ws + WS_ACT);
  for (int i = blockIdx.x * 256 + threadIdx.x; i < S_ * 4; i += 65536) {
    u32x4 z = {0, 0, 0, 0};
    *(u32x4*)(act + (size_t)(i >> 2) * DFP + DF + (i & 3) * 8) = z;
  }
}

__global__ __launch_bounds__(256) void k0b_calib(Params p) {
  __shared__ unsigned tsum[2];
  int lane = threadIdx.x & 63, wave = threadIdx.x >> 6;
  if (threadIdx.x < 2) tsum[threadIdx.x] = 0;
  __syncthreads();
  __amdgpu_buffer_rsrc_t r = __builtin_amdgcn_make_buffer_rsrc(p.ex, 0, EX_PHYS, 0x00020000);
  for (int rep = 0; rep < 4; ++rep) {
    for (int par = 0; par < 2; ++par) {
      unsigned L = EX_LOGICAL + (unsigned)(wave * 4 + rep) * 256u;
      unsigned off = exphys(L, par);
      __builtin_amdgcn_raw_buffer_store_b32(1u, r, off, 0, 16);
      asm volatile("s_waitcnt vmcnt(0)" ::: "memory");
      long long t0 = clock64();
      asm volatile("" ::: "memory");
      unsigned x = __builtin_amdgcn_raw_buffer_load_b32(r, off, 0, 16);
      asm volatile("s_waitcnt vmcnt(0)" :: "v"(x) : "memory");
      long long t1 = clock64();
      if (lane == 0) atomicAdd(&tsum[par], (unsigned)(t1 - t0));
    }
  }
  __syncthreads();
  unsigned par = tsum[1] < tsum[0] ? 1u : 0u;
  if (threadIdx.x == 0) *(unsigned*)(p.ws + WS_CTL) = par;
  for (int i = threadIdx.x; i < 512; i += 256) {
    int u = i >> 3, row = i & 7;
    u32x4 v = *(const u32x4*)(p.mem0 + row * DM + u * 8);
#pragma unroll
    for (int j = 0; j < 4; ++j) {
      unsigned lo = v[j] & 0xffffu, hi = v[j] >> 16;
      if (lo == 0xffffu) lo = 0x7fc0u;
      if (hi == 0xffffu) hi = 0x7fc0u;
      v[j] = lo | (hi << 16);
    }
    *(u32x4*)(p.ex + exphys(EX_MEM + u * 128 + row * 16, par)) = v;
  }
}

template <int BM, class BSrc>
DI void gemm_tile(const u16* A, int lda, int nk, BSrc bsrc, char* lds, f32x4 (&acc)[BM / 32][4]) {
  constexpr int MT = BM / 32;
  constexpr int ABYTES = BM * 128;
  constexpr int STAGE = ABYTES + 16384;
  const int wave = __builtin_amdgcn_readfirstlane(threadIdx.x >> 6), lane = threadIdx.x & 63;
  const int wr = wave >> 1, wc = wave & 1, g = lane >> 4, r16 = lane & 15;
  auto stage = [&](int kt, int buf) {
    char* la = lds + buf * STAGE;
    char* lb = la + ABYTES;
#pragma unroll
    for (int i = 0; i < BM / 32; ++i) {
      int blk = wave * (BM / 32) + i;
      int row = blk * 8 + (lane >> 3);
      int ch = (lane & 7) ^ (row & 7);
      glds16(A + (size_t)row * lda + kt * 64 + ch * 8, la + blk * 1024);
    }
#pragma unroll
    for (int i = 0; i < 4; ++i) {
      int blk = wave * 4 + i;
      int row = blk * 4 + (lane >> 4);
      int ch = (lane & 15) ^ (((row & 3) << 2) | ((row >> 2) & 3));
      glds16(bsrc(kt * 64 + row, ch), lb + blk * 1024);
    }
  };
  stage(0, 0);
  for (int kt = 0; kt < nk; ++kt) {
    asm volatile("s_waitcnt vmcnt(0)" ::: "memory");
    barrier_raw();
    if (kt + 1 < nk) stage(kt + 1, (kt + 1) & 1);
    const char* la = lds + (kt & 1) * STAGE;
    const char* lb = la + ABYTES;
#pragma unroll
    for (int kk = 0; kk < 2; ++kk) {
      bf16x8 af[MT], bfr[4];
#pragma unroll
      for (int m = 0; m < MT; ++m) {
        int row = wr * (BM / 2) + m * 16 + r16;
        af[m] = *(const LAS bf16x8*)(la + row * 128 + (((kk * 4 + g) ^ (row & 7)) * 16));
      }
#pragma unroll
      for (int n = 0; n < 4; ++n) {
        int c = wc * 4 + n, q = r16 >> 2, pp = lane & 3;
        s16x4 lo = tr_read(lb, off_b(32 * kk + 8 * g + q, 2 * c + (pp >> 1)) + 8 * (pp & 1));
        s16x4 hi = tr_read(lb, off_b(32 * kk + 8 * g + 4 + q, 2 * c + (pp >> 1)) + 8 * (pp & 1));
        s16x8 t = __builtin_shufflevector(lo, hi, 0, 1, 2, 3, 4, 5, 6, 7);
        bfr[n] = __builtin_bit_cast(bf16x8, t);
      }
#pragma unroll
      for (int m = 0; m < MT; ++m)
#pragma unroll
        for (int n = 0; n < 4; ++n) acc[m][n] = __builtin_amdgcn_mfma_f32_16x16x32_bf16(af[m], bfr[n], acc[m][n], 0, 0, 0);
    }
  }
  barrier_raw();
}

__global__ __launch_bounds__(256) void k1_qkv(Params p) {
  extern __shared__ __attribute__((aligned(16))) char lds[];
  int bn = blockIdx.x % 96, bm = blockIdx.x / 96;
  int which = bn >> 5, h = bn & 31;
  const u16* W = which == 0 ? p.wq : (which == 1 ? p.wk : p.wv);
  const u16* Wc = W + h * 128;
  f32x4 acc[4][4] = {};
  gemm_tile<128>((const u16*)(p.ws + WS_XN) + (size_t)bm * 128 * DM, DM, 8,
                 [&](int k, int ch) { return Wc + (size_t)k * HD + ch * 8; }, lds, acc);
  const int wave = threadIdx.x >> 6, lane = threadIdx.x & 63;
  const int wr = wave >> 1, wc = wave & 1, g = lane >> 4, r16 = lane & 15;
  if (which < 2) {
    u16* dst = (u16*)(p.ws + (which == 0 ? WS_Q : WS_K));
#pragma unroll
    for (int m = 0; m < 4; ++m)
#pragma unroll
      for (int n = 0; n < 4; ++n)
#pragma unroll
        for (int r = 0; r < 4; ++r) {
          int row = bm * 128 + wr * 64 + m * 16 + g * 4 + r, col = h * 128 + wc * 64 + n * 16 + r16;
          __bf16 v = (__bf16)acc[m][n][r];
          dst[(size_t)row * HD + col] = __builtin_bit_cast(u16, v);
        }
  } else {
    u16* dst = (u16*)(p.ws + WS_VT);
#pragma unroll
    for (int m = 0; m < 4; ++m)
#pragma unroll
      for (int n = 0; n < 4; ++n) {
        int row = bm * 128 + wr * 64 + m * 16 + g * 4;
        int seg = row >> 5, tq = (row & 31) >> 2;
        int pos = tq < 4 ? 8 * tq : 8 * (tq - 4) + 4;
        int e = wc * 64 + n * 16 + r16;
        u32x2 v = {pack2(acc[m][n][0], acc[m][n][1]), pack2(acc[m][n][2], acc[m][n][3])};
        *(u32x2*)(dst + (((size_t)seg * NH + h) * 128 + e) * 32 + pos) = v;
      }
  }
}

constexpr int L_XB = 0;
constexpr int L_QK = 8192;
constexpr int L_VX = 12288;
constexpr int L_KT = 16384;
constexpr int L_WO = 24576;
constexpr int L_WV = 32768;
constexpr int L_TOTAL = L_WV + 131072;

DI unsigned umax4(u32x4 v) { return max(max(v[0], v[1]), max(v[2], v[3])); }
DI unsigned nosent(unsigned v) { return min(v, 0xfffffffeu); }
template <class Addr>
DI bf16x16 load_bfrag(const char* lds, int lane, Addr addr) {
  const int gB = lane >> 4, q = (lane & 15) >> 2, pp = lane & 3;
  s16x4 t[4];
#pragma unroll
  for (int u = 0; u < 4; ++u) {
    int k = (u < 2) ? (8 * gB + 4 * u + q) : (32 + 8 * gB + 4 * (u - 2) + q);
    t[u] = tr_read(lds, addr(k, pp));
  }
  s16x8 a = __builtin_shufflevector(t[0], t[1], 0, 1, 2, 3, 4, 5, 6, 7);
  s16x8 b = __builtin_shufflevector(t[2], t[3], 0, 1, 2, 3, 4, 5, 6, 7);
  typedef __attribute__((ext_vector_type(16))) short s16x16;
  s16x16 c = __builtin_shufflevector(a, b, 0, 1, 2, 3, 4, 5, 6, 7, 8, 9, 10, 11, 12, 13, 14, 15);
  return __builtin_bit_cast(bf16x16, c);
}
DI void swap_add(const f32x4& a, const f32x4& b_in, f32x4& out) {
#pragma unroll
  for (int r = 0; r < 4; ++r) {
    auto sw = __builtin_amdgcn_permlane32_swap(__float_as_uint(a[r]), __float_as_uint(b_in[r]), false, false);
    out[r] = __uint_as_float(sw[0]) + __uint_as_float(sw[1]);
  }
}
DI float xmax_g(float x) {
  auto a = __builtin_amdgcn_permlane16_swap(__float_as_uint(x), __float_as_uint(x), false, false);
  x = fmaxf(__uint_as_float(a[0]), __uint_as_float(a[1]));
  auto b = __builtin_amdgcn_permlane32_swap(__float_as_uint(x), __float_as_uint(x), false, false);
  return fmaxf(__uint_as_float(b[0]), __uint_as_float(b[1]));
}
DI float xsum_g(float x) {
  auto a = __builtin_amdgcn_permlane16_swap(__float_as_uint(x), __float_as_uint(x), false, false);
  x = __uint_as_float(a[0]) + __uint_as_float(a[1]);
  auto b = __builtin_amdgcn_permlane32_swap(__float_as_uint(x), __float_as_uint(x), false, false);
  return __uint_as_float(b[0]) + __uint_as_float(b[1]);
}
#define PIN_A(x) asm volatile("" : "+a"(x))
#define PIN_V(x) asm volatile("" : "+v"(x))
#define SCHED() __builtin_amdgcn_sched_barrier(0)
#define SMF(a, b, c) __builtin_amdgcn_smfmac_f32_16x16x64_bf16((a), (b), (c), sidx, 0, 0)
#define MF(a, b, c) __builtin_amdgcn_mfma_f32_16x16x32_bf16((a), (b), (c), 0, 0, 0)
#define MFV0(acc, a, b) asm volatile("v_mfma_f32_16x16x32_bf16 %0, %1, %2, 0" : "=&v"(acc) : "v"(a), "v"(b))
#define MFV(acc, a, b) asm volatile("v_mfma_f32_16x16x32_bf16 %0, %1, %2, %0" : "+v"(acc) : "v"(a), "v"(b))

DI void chain_head(const Params& p, int h, char* lds, unsigned par) {
  const int wave = __builtin_amdgcn_readfirstlane(threadIdx.x >> 6), lane = threadIdx.x & 63;
  const int g = lane >> 4, r16 = lane & 15;
  const __amdgpu_buffer_rsrc_t rex = __builtin_amdgcn_make_buffer_rsrc(p.ex, 0, EX_PHYS, 0x00020000);
  const int sidx = r16 < 8 ? 0x4444 : 0xEEEE;
  LAS char* L = (LAS char*)lds;

  bf16x16 wq[2][8], wk[2][8], wo[4][4];
  { int s = 0; STAMP(15); }
  auto stage_qkv = [&](const u16* W) {
#pragma unroll 4
    for (int i = 0; i < 32; ++i) {
      int blk = wave * 32 + i;
      int row = blk * 4 + (lane >> 4);
      int ch = (lane & 15) ^ (((row & 3) << 2) | ((row >> 2) & 3));
      glds16(W + (size_t)row * HD + h * 128 + ch * 8, lds + L_WV + blk * 1024);
    }
    asm volatile("s_waitcnt vmcnt(0)" ::: "memory");
    barrier_raw();
  };
  auto qkv_frag = [&](int c2, int t) {
    return load_bfrag(lds + L_WV, lane, [&](int k, int pp) {
      int row = 64 * t + 16 * (k >> 4) + 8 * ((k >> 1) & 1) + 2 * ((k >> 2) & 3) + (k & 1);
      return off_b(row, 4 * wave + 2 * c2 + (pp >> 1)) + 8 * (pp & 1);
    });
  };
  stage_qkv(p.wq);
#pragma unroll
  for (int c2 = 0; c2 < 2; ++c2)
#pragma unroll
    for (int t = 0; t < 8; ++t) { wq[c2][t] = qkv_frag(c2, t); PIN_A(wq[c2][t]); }
  barrier_raw();
  stage_qkv(p.wk);
#pragma unroll
  for (int c2 = 0; c2 < 2; ++c2)
#pragma unroll
    for (int t = 0; t < 8; ++t) { wk[c2][t] = qkv_frag(c2, t); PIN_A(wk[c2][t]); }
  barrier_raw();
  stage_qkv(p.wv);
  {
    bf16x16 wvt[2][8];
#pragma unroll
    for (int c2 = 0; c2 < 2; ++c2)
#pragma unroll
      for (int t = 0; t < 8; ++t) wvt[c2][t] = qkv_frag(c2, t);
    barrier_raw();
#pragma unroll
    for (int c2 = 0; c2 < 2; ++c2)
#pragma unroll
      for (int t = 0; t < 8; ++t) {
        bf16x8 lo = __builtin_shufflevector(wvt[c2][t], wvt[c2][t], 0, 1, 2, 3, 4, 5, 6, 7);
        bf16x8 hi = __builtin_shufflevector(wvt[c2][t], wvt[c2][t], 8, 9, 10, 11, 12, 13, 14, 15);
        LAS char* d = L + L_WV + wave * 32768 + (t * 2 + c2) * 2048 + lane * 16;
        *(LAS bf16x8*)d = lo;
        *(LAS bf16x8*)(d + 1024) = hi;
      }
  }
#pragma unroll
  for (int t2 = 0; t2 < 4; ++t2) {
    char* st = lds;
#pragma unroll
    for (int i = 0; i < 8; ++i) {
      int row = wave * 8 + i;
      int ch = lane ^ ((row & 7) << 1);
      glds16(p.wo + ((size_t)h * 128 + t2 * 32 + row) * DM + ch * 8, st + row * 1024);
    }
    asm volatile("s_waitcnt vmcnt(0)" ::: "memory");
    barrier_raw();
#pragma unroll
    for (int cp = 0; cp < 4; ++cp) {
      wo[cp][t2] = load_bfrag(st, lane, [&](int k, int pp) {
        int mm = (k >> 2) & 3;
        int row = 16 * (mm >> 1) + 4 * (k >> 4) + 2 * (mm & 1) + (k & 1);
        int ch = 16 * wave + 4 * cp + 2 * ((k >> 1) & 1) + (pp >> 1);
        return row * 1024 + ((ch ^ ((row & 7) << 1)) * 16) + 8 * (pp & 1);
      });
      PIN_V(wo[cp][t2]);
    }
    barrier_raw();
  }
  {
    bf16x8 lo = __builtin_shufflevector(wo[3][3], wo[3][3], 0, 1, 2, 3, 4, 5, 6, 7);
    bf16x8 hi = __builtin_shufflevector(wo[3][3], wo[3][3], 8, 9, 10, 11, 12, 13, 14, 15);
    *(LAS bf16x8*)(L + L_WO + wave * 2048 + lane * 16) = lo;
    *(LAS bf16x8*)(L + L_WO + wave * 2048 + 1024 + lane * 16) = hi;
  }
  const __amdgpu_buffer_rsrc_t rk = __builtin_amdgcn_make_buffer_rsrc(p.ws + WS_K, 0, S_ * HD * 2, 0x00020000);
  const __amdgpu_buffer_rsrc_t rv = __builtin_amdgcn_make_buffer_rsrc(p.ws + WS_VT, 0, S_ * HD * 2, 0x00020000);
  unsigned kvo[2];
#pragma unroll
  for (int i = 0; i < 2; ++i) {
    int key = (wave * 2 + i) * 4 + (lane >> 4);
    kvo[i] = (unsigned)((key * HD + h * 128 + (((lane & 15) ^ (key & 15)) * 8)) * 2);
  }
  const unsigned vto = (unsigned)(((h * 128 + wave * 32 + r16) * 32 + g * 8) * 2);
  auto stage_k = [&](int s) {
#pragma unroll
    for (int i = 0; i < 2; ++i)
      __builtin_amdgcn_raw_ptr_buffer_load_lds(rk, L + L_KT + (wave * 2 + i) * 1024, 16, kvo[i], s * (32 * HD * 2), 0, 0);
  };
  stage_k(0);

  const __amdgpu_buffer_rsrc_t rkm = __builtin_amdgcn_make_buffer_rsrc(p.ws + WS_KM, 0, NSEG * NH * 2048, 0x00020000);
  const __amdgpu_buffer_rsrc_t rvm = __builtin_amdgcn_make_buffer_rsrc(p.ws + WS_VM, 0, NSEG * NH * 4096, 0x00020000);
  const float sc = 0.08838834764831845f * 1.4426950408889634f;
  const unsigned lane16 = lane * 16;
  const float gl2 = g < 2 ? 1.f : 0.f;
  LAS char* xb_w = L + L_XB + wave * 2048 + lane16;
  LAS char* xb_r = L + L_XB + lane16;
  LAS char* wv_r = L + L_WV + wave * 32768 + lane16;
  LAS char* qk_w = L + L_QK + (wave * 32 + (lane >> 5) * 16 + r16) * 32 + (g & 1) * 8;
  LAS char* qk_r = L + L_QK + (g * 8 + (r16 >> 2)) * 32 + (lane & 1) * 8;
  LAS char* ox_w = L + L_VX + wave * 1024 + lane16;
  LAS char* ox_r = L + L_VX + lane16;
  LAS char* kt_r[4];
#pragma unroll
  for (int kk = 0; kk < 4; ++kk) kt_r[kk] = L + r16 * 256 + (((kk * 4 + g) ^ r16) * 16);
  const unsigned part_off = (unsigned)((wave * 128 + (g >> 1) * 16 + r16) * 16 + (g & 1) * 8);
  const unsigned km_off = r16 < 8 ? (unsigned)((r16 * 128 + wave * 32 + g * 8) * 2) : 0x7fffffffu;
  auto wvfrag = [&](int i) {
    bf16x8 lo = *(const LAS bf16x8*)(wv_r + i * 2048), hi = *(const LAS bf16x8*)(wv_r + i * 2048 + 1024);
    return __builtin_shufflevector(lo, hi, 0, 1, 2, 3, 4, 5, 6, 7, 8, 9, 10, 11, 12, 13, 14, 15);
  };
  barrier_raw();

  for (int s = 0; s < NSEG; ++s) {
    bf16x16 wv[16];
    bf16x8 va[8];
#define WLOAD(t) do { wv[2 * (t)] = wvfrag(2 * (t)); wv[2 * (t) + 1] = wvfrag(2 * (t) + 1); } while (0)
#define VLOAD(t) do { WLOAD(t); va[t] = *(const LAS bf16x8*)(xb_r + (t) * 1024); } while (0)
#define VSTEP(t) do { av0 = SMF(va[t], wv[2 * (t)], av0); av1 = SMF(va[t], wv[2 * (t) + 1], av1); asm volatile("" : "+v"(av0), "+v"(av1)); } while (0)
    WLOAD(0); WLOAD(1); WLOAD(2);
    u32x4 vt0 = __builtin_amdgcn_raw_buffer_load_b128(rv, vto, s * (NH * 128 * 32 * 2), 0);
    u32x4 vt1 = __builtin_amdgcn_raw_buffer_load_b128(rv, vto + 1024, s * (NH * 128 * 32 * 2), 0);
    asm volatile("" : "+v"(wv[0]), "+v"(wv[1]), "+v"(wv[2]), "+v"(wv[3]), "+v"(wv[4]), "+v"(wv[5]));
    f32x4 z4 = {0.f, 0.f, 0.f, 0.f};
    f32x4 aq0 = z4, aq1 = z4, ak0 = z4, ak1 = z4, av0 = z4, av1 = z4;
    asm volatile("" : "+v"(aq0), "+v"(aq1), "+v"(ak0), "+v"(ak1), "+v"(av0), "+v"(av1));
    {
      u32x4 f0, f1;
      const unsigned so = exphys(EX_MEM + s * 8192, par) + wave * 2048;
      unsigned sp = 0;
      for (;;) {
        asm volatile("" ::: "memory");
        f0 = __builtin_amdgcn_raw_buffer_load_b128(rex, lane16, so, 16);
        f1 = __builtin_amdgcn_raw_buffer_load_b128(rex, lane16 + 1024, so, 16);
        unsigned mxv = max(umax4(f0), umax4(f1));
        if (__all(mxv != 0xffffffffu)) break;
        if (++sp > (1u << 22)) break;
      }
      STAMP(0);
      *(LAS u32x4*)(xb_w) = f0;
      *(LAS u32x4*)(xb_w + 1024) = f1;
    }
    barrier_raw();
    STAMP(1);
    bf16x8 af[8];
#define AFLOAD(t) af[t] = *(const LAS bf16x8*)(xb_r + (t) * 1024)
#define QKSTEP(t) do { aq0 = SMF(af[t], wq[0][t], aq0); aq1 = SMF(af[t], wq[1][t], aq1); ak0 = SMF(af[t], wk[0][t], ak0); ak1 = SMF(af[t], wk[1][t], ak1); \
                       asm volatile("" : "+v"(aq0), "+v"(aq1), "+v"(ak0), "+v"(ak1)); } while (0)
    AFLOAD(3); AFLOAD(4); AFLOAD(5); AFLOAD(6); SCHED();
    QKSTEP(3); AFLOAD(7); SCHED();
    QKSTEP(4); AFLOAD(0); SCHED();
    QKSTEP(5); AFLOAD(1); SCHED();
    QKSTEP(6); AFLOAD(2); SCHED();
    QKSTEP(7); QKSTEP(0); QKSTEP(1); QKSTEP(2);
    va[0] = af[0]; va[1] = af[1]; va[2] = af[2];
    SCHED();
    VSTEP(0);
    {
      f32x4 sq, sk;
      swap_add(aq0, aq1, sq);
      swap_add(ak0, ak1, sk);
      u32x2 vq = {pack2(sq[0], sq[1]), pack2(sq[2], sq[3])};
      u32x2 vk = {pack2(sk[0], sk[1]), pack2(sk[2], sk[3])};
      *(LAS u32x2*)(qk_w) = vq;
      *(LAS u32x2*)(qk_w + 16) = vk;
    }
    SCHED();
    bf16x8 qf[4], kf[4], k1[4], k2[4];
#pragma unroll
    for (int kk = 0; kk < 2; ++kk) {
      k1[kk] = *(const LAS bf16x8*)(kt_r[kk] + L_KT);
      k2[kk] = *(const LAS bf16x8*)(kt_r[kk] + L_KT + 4096);
    }
    VSTEP(1);
    STAMP(2);
    barrier_raw();
    STAMP(3);
#define QKLOAD(kk) do { \
      s16x4 ql = tr_read_l(qk_r + (kk) * 1024), qh = tr_read_l(qk_r + (kk) * 1024 + 128); \
      qf[kk] = __builtin_bit_cast(bf16x8, __builtin_shufflevector(ql, qh, 0, 1, 2, 3, 4, 5, 6, 7)); \
      s16x4 kl = tr_read_l(qk_r + (kk) * 1024 + 16), kh = tr_read_l(qk_r + (kk) * 1024 + 144); \
      kf[kk] = __builtin_bit_cast(bf16x8, __builtin_shufflevector(kl, kh, 0, 1, 2, 3, 4, 5, 6, 7)); } while (0)
    QKLOAD(0); QKLOAD(1); QKLOAD(2); QKLOAD(3);
    VSTEP(2);
    SCHED();
#pragma unroll
    for (int kk = 2; kk < 4; ++kk) {
      k1[kk] = *(const LAS bf16x8*)(kt_r[kk] + L_KT);
      k2[kk] = *(const LAS bf16x8*)(kt_r[kk] + L_KT + 4096);
    }
    SCHED();
    f32x4 sm, s1, s2;
    MFV0(s1, k1[0], qf[0]); MFV0(s2, k2[0], qf[0]); MFV0(sm, kf[0], qf[0]);
    MFV(s1, k1[1], qf[1]); MFV(s2, k2[1], qf[1]); MFV(sm, kf[1], qf[1]);
    SCHED(); VLOAD(3);
    MFV(s1, k1[2], qf[2]); MFV(s2, k2[2], qf[2]); MFV(sm, kf[2], qf[2]);
    MFV(s1, k1[3], qf[3]); MFV(s2, k2[3], qf[3]); MFV(sm, kf[3], qf[3]);
    SCHED(); VLOAD(4);
    VLOAD(5);
    VSTEP(3);
    asm volatile("s_nop 0" : "+v"(s1), "+v"(s2), "+v"(sm));
    STAMP(4);
    float mx;
    {
      float m0 = __builtin_fmaxf(__builtin_fmaxf(sm[0], sm[1]), sm[2]);
      float m1 = __builtin_fmaxf(__builtin_fmaxf(sm[3], s1[0]), s1[1]);
      float m2 = __builtin_fmaxf(__builtin_fmaxf(s1[2], s1[3]), s2[0]);
      float m3 = __builtin_fmaxf(__builtin_fmaxf(s2[1], s2[2]), s2[3]);
      asm("v_max3_f32 %0, %1, %2, %3" : "=v"(m0) : "v"(m0), "v"(m1), "v"(m2));
      asm("v_max_f32 %0, %1, %2" : "=v"(mx) : "v"(m0), "v"(m3));
    }
    VLOAD(6);
    {
      auto a = __builtin_amdgcn_permlane16_swap(__float_as_uint(mx), __float_as_uint(mx), false, false);
      asm("v_max_f32 %0, %1, %2" : "=v"(mx) : "v"(a[0]), "v"(a[1]));
      VSTEP(4);
      auto b = __builtin_amdgcn_permlane32_swap(__float_as_uint(mx), __float_as_uint(mx), false, false);
      asm("v_max_f32 %0, %1, %2" : "=v"(mx) : "v"(b[0]), "v"(b[1]));
    }
    VLOAD(7);
    SCHED();
    float nm = -mx * sc;
    {
      f32x4 scv = {sc, sc, sc, sc}, nmv = {nm, nm, nm, nm};
      s1 = s1 * scv + nmv; s2 = s2 * scv + nmv; sm = sm * scv + nmv;
    }
#pragma unroll
    for (int r = 0; r < 4; ++r) {
      s1[r] = __builtin_amdgcn_exp2f(s1[r]);
      s2[r] = __builtin_amdgcn_exp2f(s2[r]);
    }
    VSTEP(5);
    u32x4 ptk = {pack2(s1[0], s1[1]), pack2(s1[2], s1[3]), pack2(s2[0], s2[1]), pack2(s2[2], s2[3])};
    bf16x8 pt = __builtin_bit_cast(bf16x8, ptk);
#pragma unroll
    for (int r = 0; r < 4; ++r) sm[r] = __builtin_amdgcn_exp2f(sm[r]);
    VSTEP(6);
    VSTEP(7);
    f32x4 ao0, ao1;
    asm volatile("" : "+v"(pt));
    MFV0(ao0, __builtin_bit_cast(bf16x8, vt0), pt);
    MFV0(ao1, __builtin_bit_cast(bf16x8, vt1), pt);
    unsigned pm0 = pack2(sm[0], sm[1]), pm1 = pack2(sm[2], sm[3]);
    u32x4 pml4 = {pm0, pm1, 0u, 0u}, pmh4 = {0u, 0u, pm0, pm1};
    bf16x8 pml = __builtin_bit_cast(bf16x8, pml4), pmh = __builtin_bit_cast(bf16x8, pmh4);
    u32x4 vv = {pack2(av0[0], av0[1]), pack2(av0[2], av0[3]), pack2(av1[0], av1[1]), pack2(av1[2], av1[3])};
    bf16x8 vvb = __builtin_bit_cast(bf16x8, vv);
    asm volatile("s_nop 1" : "+v"(vvb), "+v"(pml), "+v"(pmh));
    MFV(ao0, vvb, pml);
    MFV(ao1, vvb, pmh);
    float lsum;
    {
      f32x4 t = s1 + s2;
      f32x4 msk = {gl2, gl2, gl2, gl2};
      t = sm * msk + t;
      lsum = (t[0] + t[1]) + (t[2] + t[3]);
    }
    lsum = xsum_g(lsum);
    float inv = __builtin_amdgcn_rcpf(lsum);
    if (s < NSEG - 1) {
      asm volatile("s_nop 1" : "+v"(ao0), "+v"(ao1), "+v"(inv));
      STAMP(5);
      {
        f32x4 iv = {inv, inv, inv, inv};
        ao0 *= iv; ao1 *= iv;
        u32x4 w = {pack2(ao0[0], ao0[1]), pack2(ao0[2], ao0[3]), pack2(ao1[0], ao1[1]), pack2(ao1[2], ao1[3])};
        *(LAS u32x4*)(ox_w) = w;
      }
      bf16x16 wo33;
      {
        bf16x8 lo = *(const LAS bf16x8*)(xb_w + L_WO), hi = *(const LAS bf16x8*)(xb_w + L_WO + 1024);
        wo33 = __builtin_shufflevector(lo, hi, 0, 1, 2, 3, 4, 5, 6, 7, 8, 9, 10, 11, 12, 13, 14, 15);
      }
      f32x4 z5 = {0.f, 0.f, 0.f, 0.f};
      f32x4 ap[4] = {z5, z5, z5, z5};
      asm volatile("" : "+v"(ap[0]), "+v"(ap[1]), "+v"(ap[2]), "+v"(ap[3]));
      barrier_raw();
      STAMP(6);
      bf16x8 of[4];
#pragma unroll
      for (int t2 = 0; t2 < 4; ++t2) of[t2] = *(const LAS bf16x8*)(ox_r + t2 * 1024);
      SCHED();
      const unsigned so = exphys(EX_PART + ((unsigned)s * 32 + h) * 8192, par);
#pragma unroll
      for (int t2 = 0; t2 < 3; ++t2)
#pragma unroll
        for (int cp = 0; cp < 4; ++cp) ap[cp] = SMF(of[t2], wo[cp][t2], ap[cp]);
#pragma unroll
      for (int cp = 0; cp < 4; ++cp) ap[cp] = SMF(of[3], cp == 3 ? wo33 : wo[cp][3], ap[cp]);
#pragma unroll
      for (int cp = 0; cp < 4; ++cp) {
        u32x2 v = {nosent(pack2(ap[cp][0], ap[cp][1])), nosent(pack2(ap[cp][2], ap[cp][3]))};
        __builtin_amdgcn_raw_buffer_store_b64(v, rex, part_off + cp * 512, so, 16);
      }
      SCHED();
      STAMP(7);
      stage_k(s + 1);
    }
    {
      s16x4 kl = tr_read_l(qk_r + wave * 1024 + 16), kh = tr_read_l(qk_r + wave * 1024 + 144);
      s16x8 kp = __builtin_shufflevector(kl, kh, 0, 1, 2, 3, 4, 5, 6, 7);
      __builtin_amdgcn_raw_buffer_store_b128(__builtin_bit_cast(u32x4, kp), rkm, km_off, (s * NH + h) * 2048, 0);
      __builtin_amdgcn_raw_buffer_store_b128(__builtin_bit_cast(u32x4, vvb), rvm, lane16 + wave * 1024, (s * NH + h) * 4096, 0);
    }
    STAMP(8);
  }
}

DI void chain_reducer(const Params& p, int unit, unsigned par) {
  const int lane = threadIdx.x & 63;
  const __amdgpu_buffer_rsrc_t rex = __builtin_amdgcn_make_buffer_rsrc(p.ex, 0, EX_PHYS, 0x00020000);
  const int hg = lane >> 3, q = lane & 7;
  const unsigned lo = (unsigned)(hg * 4 * 16384 + unit * 128 + q * 16);
  const int row = ((lane >> 5) & 1) * 4 + ((lane >> 4) & 1) * 2 + ((lane >> 3) & 1);
  const unsigned oo = (q & 1) ? 0x7fffffffu : (unsigned)(unit * 128 + row * 16 + q * 2);
  for (int s = 0; s < NSEG - 1; ++s) {
    u32x4 v[4];
    unsigned sp = 0;
    const unsigned so = exphys(EX_PART + (unsigned)s * 32 * 8192, par);
    for (;;) {
      asm volatile("" ::: "memory");
#pragma unroll
      for (int k = 0; k < 4; ++k) v[k] = __builtin_amdgcn_raw_buffer_load_b128(rex, lo + k * 16384, so, 16);
      unsigned mxv = max(max(umax4(v[0]), umax4(v[1])), max(umax4(v[2]), umax4(v[3])));
      if (__all(mxv != 0xffffffffu)) break;
      if (++sp > (1u << 22)) break;
    }
    float a[8];
#pragma unroll
    for (int j = 0; j < 4; ++j) {
      a[2 * j] = (__uint_as_float(v[0][j] << 16) + __uint_as_float(v[1][j] << 16)) + (__uint_as_float(v[2][j] << 16) + __uint_as_float(v[3][j] << 16));
      a[2 * j + 1] = (__uint_as_float(v[0][j] & 0xffff0000u) + __uint_as_float(v[1][j] & 0xffff0000u)) + (__uint_as_float(v[2][j] & 0xffff0000u) + __uint_as_float(v[3][j] & 0xffff0000u));
    }
    float b[4];
#pragma unroll
    for (int j = 0; j < 4; ++j) {
      auto sw = __builtin_amdgcn_permlane32_swap(__float_as_uint(a[j]), __float_as_uint(a[4 + j]), false, false);
      b[j] = __uint_as_float(sw[0]) + __uint_as_float(sw[1]);
    }
    float c[2];
#pragma unroll
    for (int j = 0; j < 2; ++j) {
      auto sw = __builtin_amdgcn_permlane16_swap(__float_as_uint(b[j]), __float_as_uint(b[2 + j]), false, false);
      c[j] = __uint_as_float(sw[0]) + __uint_as_float(sw[1]);
    }
    float d;
    {
      bool up = (lane & 8) != 0;
      float send = up ? c[0] : c[1];
      float keep = up ? c[1] : c[0];
      int t = __builtin_amdgcn_update_dpp(0, __float_as_int(send), 0x128, 0xf, 0xf, false);
      d = keep + __int_as_float(t);
    }
    int dn = __builtin_amdgcn_update_dpp(0, __float_as_int(d), 0xB1, 0xf, 0xf, false);
    unsigned ob = nosent(pack2(d, __int_as_float(dn)));
    __builtin_amdgcn_raw_buffer_store_b32(ob, rex, oo, exphys(EX_MEM + (unsigned)(s + 1) * 8192, par), 16);
  }
}

__global__ __launch_bounds__(256, 1) void k2_chain(Params p) {
  extern __shared__ __attribute__((aligned(16))) char lds[];
  const int b = blockIdx.x;
  const unsigned par = *(const unsigned*)(p.ws + WS_CTL);
  const int x = b & 7, c = b >> 3;
  if (x < 4 && c < 8) chain_head(p, c * 4 + x, lds, par);
  else if (x < 4 && c < 16) {
    int wave = threadIdx.x >> 6;
    if (wave < 2) chain_reducer(p, ((c - 8) * 4 + x) * 2 + wave, par);
  }
}

__global__ __launch_bounds__(256) void k3_attn(Params p) {
  const int wave = threadIdx.x >> 6, lane = threadIdx.x & 63;
  const int g = lane >> 4, r16 = lane & 15;
  int id = blockIdx.x * 4 + wave;
  int qt = id & 1, h = (id >> 1) & 31, s = id >> 6;
  const u16* Q = (const u16*)(p.ws + WS_Q);
  const u16* K = (const u16*)(p.ws + WS_K);
  const u16* VT = (const u16*)(p.ws + WS_VT) + ((size_t)s * NH + h) * 128 * 32;
  const u16* KM = (const u16*)(p.ws + WS_KM) + ((size_t)s * NH + h) * 1024;
  const u16* VM = (const u16*)(p.ws + WS_VM) + ((size_t)s * NH + h) * 2048;
  f32x4 sm = {}, s1 = {}, s2 = {};
#pragma unroll
  for (int kk = 0; kk < 4; ++kk) {
    int eo = h * 128 + kk * 32 + g * 8;
    bf16x8 qf = *(const bf16x8*)(Q + ((size_t)s * 32 + qt * 16 + r16) * HD + eo);
    bf16x8 k1 = *(const bf16x8*)(K + ((size_t)s * 32 + r16) * HD + eo);
    bf16x8 k2 = *(const bf16x8*)(K + ((size_t)s * 32 + 16 + r16) * HD + eo);
    bf16x8 km = *(const bf16x8*)(KM + (r16 & 7) * 128 + kk * 32 + g * 8);
    sm = __builtin_amdgcn_mfma_f32_16x16x32_bf16(km, qf, sm, 0, 0, 0);
    s1 = __builtin_amdgcn_mfma_f32_16x16x32_bf16(k1, qf, s1, 0, 0, 0);
    s2 = __builtin_amdgcn_mfma_f32_16x16x32_bf16(k2, qf, s2, 0, 0, 0);
  }
  const float sc = 0.08838834764831845f * 1.4426950408889634f;
  int qi = qt * 16 + r16;
  const float NEG = -3.0e38f;
#pragma unroll
  for (int r = 0; r < 4; ++r) {
    if (g * 4 + r > qi) s1[r] = NEG;
    if (16 + g * 4 + r > qi) s2[r] = NEG;
  }
  float mx = fmaxf(fmaxf(fmaxf(sm[0], sm[1]), fmaxf(sm[2], sm[3])), fmaxf(fmaxf(fmaxf(s1[0], s1[1]), fmaxf(s1[2], s1[3])), fmaxf(fmaxf(s2[0], s2[1]), fmaxf(s2[2], s2[3]))));
  mx = fmaxf(mx, __shfl_xor(mx, 16));
  mx = fmaxf(mx, __shfl_xor(mx, 32));
  float nm = -mx * sc;
  float lsum = 0.f, lm = 0.f;
#pragma unroll
  for (int r = 0; r < 4; ++r) {
    sm[r] = __builtin_amdgcn_exp2f(sm[r] * sc + nm); lm += sm[r];
    s1[r] = (g * 4 + r > qi) ? 0.f : __builtin_amdgcn_exp2f(s1[r] * sc + nm); lsum += s1[r];
    s2[r] = (16 + g * 4 + r > qi) ? 0.f : __builtin_amdgcn_exp2f(s2[r] * sc + nm); lsum += s2[r];
  }
  lsum += (g < 2) ? lm : 0.f;
  lsum += __shfl_xor(lsum, 16);
  lsum += __shfl_xor(lsum, 32);
  float inv = 1.0f / lsum;
  u32x4 ptk = {pack2(s1[0] * inv, s1[1] * inv), pack2(s1[2] * inv, s1[3] * inv), pack2(s2[0] * inv, s2[1] * inv), pack2(s2[2] * inv, s2[3] * inv)};
  u32x2 pmm = {pack2(sm[0] * inv, sm[1] * inv), pack2(sm[2] * inv, sm[3] * inv)};
  bf16x8 pt = __builtin_bit_cast(bf16x8, ptk);
  u32x4 pml4 = {pmm[0], pmm[1], 0u, 0u}, pmh4 = {0u, 0u, pmm[0], pmm[1]};
  bf16x8 pml = __builtin_bit_cast(bf16x8, pml4), pmh = __builtin_bit_cast(bf16x8, pmh4);
  u16* O = (u16*)(p.ws + WS_O) + ((size_t)s * 32 + qi) * HD + h * 128;
#pragma unroll
  for (int m = 0; m < 8; ++m) {
    bf16x8 vt = *(const bf16x8*)(VT + (m * 16 + r16) * 32 + g * 8);
    bf16x8 vm = *(const bf16x8*)(VM + ((m >> 1) * 64 + lane) * 8);
    f32x4 z = {0.f, 0.f, 0.f, 0.f};
    f32x4 o = __builtin_amdgcn_mfma_f32_16x16x32_bf16(vt, pt, z, 0, 0, 0);
    o = __builtin_amdgcn_mfma_f32_16x16x32_bf16(vm, (m & 1) ? pmh : pml, o, 0, 0, 0);
    u32x2 v = {pack2(o[0], o[1]), pack2(o[2], o[3])};
    *(u32x2*)(O + m * 16 + g * 4) = v;
  }
}

__global__ __launch_bounds__(256) void k4_wo(Params p) {
  extern __shared__ __attribute__((aligned(16))) char lds[];
  int bn = blockIdx.x & 3, bm = blockIdx.x >> 2;
  f32x4 acc[2][4] = {};
  const u16* Wc = p.wo + bn * 128;
  gemm_tile<64>((const u16*)(p.ws + WS_O) + (size_t)bm * 64 * HD, HD, 64,
                [&](int k, int ch) { return Wc + (size_t)k * DM + ch * 8; }, lds, acc);
  const int wave = threadIdx.x >> 6, lane = threadIdx.x & 63;
  const int wr = wave >> 1, wc = wave & 1, g = lane >> 4, r16 = lane & 15;
  float* Hb = (float*)(p.ws + WS_H);
#pragma unroll
  for (int m = 0; m < 2; ++m)
#pragma unroll
    for (int n = 0; n < 4; ++n)
#pragma unroll
      for (int r = 0; r < 4; ++r) {
        int row = bm * 64 + wr * 32 + m * 16 + g * 4 + r, col = bn * 128 + wc * 64 + n * 16 + r16;
        Hb[(size_t)row * DM + col] = bf2f(p.x[(size_t)row * DM + col]) + acc[m][n][r];
      }
}

__global__ __launch_bounds__(256) void k5_ln2(Params p) {
  int wave = threadIdx.x >> 6, lane = threadIdx.x & 63;
  int r = blockIdx.x * 4 + wave;
  const float* Hb = (const float*)(p.ws + WS_H) + (size_t)r * DM + lane * 8;
  f32x4 a = *(const f32x4*)Hb, b = *(const f32x4*)(Hb + 4);
  float v[8] = {a[0], a[1], a[2], a[3], b[0], b[1], b[2], b[3]};
  float mean, rstd; ln_row(v, mean, rstd);
  u32x4 o;
#pragma unroll
  for (int j = 0; j < 4; ++j) o[j] = pack2((v[2 * j] - mean) * rstd, (v[2 * j + 1] - mean) * rstd);
  *(u32x4*)((u16*)(p.ws + WS_HN) + (size_t)r * DM + lane * 8) = o;
}

DI float gelu_tanh(float x) {
  float u = 0.7978845608028654f * (x + 0.044715f * x * x * x);
  float e = __expf(2.0f * u);
  float t = 1.0f - 2.0f / (e + 1.0f);
  return 0.5f * x * (1.0f + t);
}

__global__ __launch_bounds__(256) void k5_ffn1(Params p) {
  extern __shared__ __attribute__((aligned(16))) char lds[];
  int bn = blockIdx.x % 22, bm = blockIdx.x / 22;
  f32x4 acc[4][4] = {};
  gemm_tile<128>((const u16*)(p.ws + WS_HN) + (size_t)bm * 128 * DM, DM, 8,
                 [&](int k, int ch) {
                   int wcx = ch >> 3, up = (ch >> 2) & 1, f = bn * 64 + wcx * 32 + (ch & 3) * 8;
                   if (f >= DF) f = 0;
                   return (up ? p.wu : p.wg) + (size_t)k * DF + f;
                 }, lds, acc);
  const int wave = threadIdx.x >> 6, lane = threadIdx.x & 63;
  const int wr = wave >> 1, wc = wave & 1, g = lane >> 4, r16 = lane & 15;
  u16* act = (u16*)(p.ws + WS_ACT);
#pragma unroll
  for (int m = 0; m < 4; ++m)
#pragma unroll
    for (int n = 0; n < 2; ++n)
#pragma unroll
      for (int r = 0; r < 4; ++r) {
        int row = bm * 128 + wr * 64 + m * 16 + g * 4 + r, f = bn * 64 + wc * 32 + n * 16 + r16;
        if (f < DF) {
          float gv = acc[m][n][r], uv = acc[m][n + 2][r];
          __bf16 v = (__bf16)(gelu_tanh(gv) * uv);
          act[(size_t)row * DFP + f] = __builtin_bit_cast(u16, v);
        }
      }
}

__global__ __launch_bounds__(256) void k6_ffn2(Params p) {
  extern __shared__ __attribute__((aligned(16))) char lds[];
  int bn = blockIdx.x & 3, bm = blockIdx.x >> 2;
  f32x4 acc[2][4] = {};
  const u16* Wc = p.wd + bn * 128;
  gemm_tile<64>((const u16*)(p.ws + WS_ACT) + (size_t)bm * 64 * DFP, DFP, 22,
                [&](int k, int ch) { int kc = k < DF ? k : 0; return Wc + (size_t)kc * DM + ch * 8; }, lds, acc);
  const int wave = threadIdx.x >> 6, lane = threadIdx.x & 63;
  const int wr = wave >> 1, wc = wave & 1, g = lane >> 4, r16 = lane & 15;
  const float* Hb = (const float*)(p.ws + WS_H);
#pragma unroll
  for (int m = 0; m < 2; ++m)
#pragma unroll
    for (int n = 0; n < 4; ++n)
#pragma unroll
      for (int r = 0; r < 4; ++r) {
        int row = bm * 64 + wr * 32 + m * 16 + g * 4 + r, col = bn * 128 + wc * 64 + n * 16 + r16;
        __bf16 v = (__bf16)(Hb[(size_t)row * DM + col] + acc[m][n][r]);
        p.out[(size_t)row * DM + col] = __builtin_bit_cast(u16, v);
      }
}

extern "C" void kernel_launch(void* const* d_in, const int* in_sizes, int n_in, void* d_out, int out_size, void* d_ws,
                              size_t ws_size, hipStream_t stream) {
  Params p;
  p.x = (const u16*)d_in[0]; p.mem0 = (const u16*)d_in[1]; p.wq = (const u16*)d_in[2]; p.wk = (const u16*)d_in[3];
  p.wv = (const u16*)d_in[4]; p.wo = (const u16*)d_in[5];
  p.wg = (const u16*)d_in[8]; p.wu = (const u16*)d_in[9]; p.wd = (const u16*)d_in[10];
  p.out = (u16*)d_out; p.ws = (char*)d_ws;
  uintptr_t exa = ((uintptr_t)d_ws + WS_EX_MIN + ((2u << 20) - 1)) & ~(uintptr_t)((2u << 20) - 1);
  p.ex = (char*)exa;
  hipFuncSetAttribute((const void*)k1_qkv, hipFuncAttributeMaxDynamicSharedMemorySize, 65536);
  hipFuncSetAttribute((const void*)k2_chain, hipFuncAttributeMaxDynamicSharedMemorySize, L_TOTAL);
  hipFuncSetAttribute((const void*)k5_ffn1, hipFuncAttributeMaxDynamicSharedMemorySize, 65536);
  k0_prologue<<<256, 256, 0, stream>>>(p);
  k0b_calib<<<1, 256, 0, stream>>>(p);
  k1_qkv<<<16 * 96, 256, 65536, stream>>>(p);
  k2_chain<<<256, 256, L_TOTAL, stream>>>(p);
  k3_attn<<<1024, 256, 0, stream>>>(p);
  k4_wo<<<128, 256, 49152, stream>>>(p);
  k5_ln2<<<512, 256, 0, stream>>>(p);
  k5_ffn1<<<16 * 22, 256, 65536, stream>>>(p);
  k6_ffn2<<<128, 256, 49152, stream>>>(p);
}
```

```cpp
#include <hip/hip_runtime.h>
#include <hip/hip_bf16.h>
#include <stdint.h>

constexpr int S_ = 2048, DM = 512, NH = 32, HD = 4096, NSEG = 64, DF = 1376, DFP = 1408;

typedef __attribute__((ext_vector_type(8))) __bf16 bf16x8;
typedef __attribute__((ext_vector_type(16))) __bf16 bf16x16;
typedef __attribute__((ext_vector_type(2))) __bf16 bf16x2;
typedef __attribute__((ext_vector_type(4))) short s16x4;
typedef __attribute__((ext_vector_type(8))) short s16x8;
typedef __attribute__((ext_vector_type(16))) short s16x16;
typedef __attribute__((ext_vector_type(4))) float f32x4;
typedef __attribute__((ext_vector_type(4))) unsigned u32x4;
typedef __attribute__((ext_vector_type(2))) unsigned u32x2;
typedef unsigned short u16;
typedef __amdgpu_buffer_rsrc_t rsrc_t;
#define LAS __attribute__((address_space(3)))
#define DI __device__ __forceinline__
template <int N> struct IC { static constexpr int v = N; };

constexpr size_t MB = 1u << 20;
constexpr size_t WS_XN = 0;
constexpr size_t WS_Q = 2 * MB;
constexpr size_t WS_K = WS_Q + 16 * MB;
constexpr size_t WS_VW = WS_K + 16 * MB;
constexpr size_t WS_VO = WS_VW + 64 * MB;
constexpr size_t WS_PT = WS_VO + 16 * MB;
constexpr size_t WS_H = WS_PT + 4 * MB;
constexpr size_t WS_ACT = WS_H + 4 * MB;
constexpr size_t WS_DBG = WS_ACT + 6 * MB;
constexpr size_t WS_EX_MIN = WS_DBG + 2 * MB;
constexpr unsigned EX_VO = 0;
constexpr unsigned EX_P = 524288;
constexpr unsigned EX_MEM = EX_P + 131072;
constexpr unsigned EX_CAL = EX_MEM + 32768;
constexpr unsigned EX_LEND = EX_CAL + 8192;
constexpr unsigned EX_FLAGS = 2 * EX_LEND;
constexpr unsigned F_PAR = 0;
constexpr unsigned F_XN = 128;
constexpr unsigned F_QK = 256;
constexpr unsigned F_VW = F_QK + 16384;
constexpr unsigned F_PT = F_VW + 16384;
constexpr unsigned F_VO = F_PT + 16384;
constexpr unsigned F_H = F_VO + 32768;
constexpr unsigned F_ACT = F_H + 8192;
constexpr unsigned F_END = F_ACT + 8192;
constexpr unsigned EX_TOTAL = EX_FLAGS + F_END;
constexpr int L_TOTAL = 160 * 1024;
constexpr unsigned OOB = 0x7fffffffu;

DI unsigned exphys(unsigned L, unsigned par) { return ((L >> 13) << 14) | (L & 8191u) | (par << 13); }
DI unsigned pack2(float a, float b) {
  bf16x2 v; v[0] = (__bf16)a; v[1] = (__bf16)b;
  return __builtin_bit_cast(unsigned, v);
}
DI unsigned off_b(unsigned row, unsigned ch) { return 256u * row + 16u * (ch ^ (((row & 3) << 2) | ((row >> 2) & 3))); }
DI s16x4 tr_read_l(LAS char* a) { return __builtin_amdgcn_ds_read_tr16_b64_v4i16((LAS s16x4*)a); }
DI bf16x8 tr_frag(LAS char* a0, LAS char* a1) {
  s16x4 lo = tr_read_l(a0), hi = tr_read_l(a1);
  return __builtin_bit_cast(bf16x8, __builtin_shufflevector(lo, hi, 0, 1, 2, 3, 4, 5, 6, 7));
}
DI void glds(const void* g, unsigned lds_dst) {
  unsigned keep;
  asm volatile("s_mov_b32 %0, m0\n\ts_mov_b32 m0, %2\n\ts_nop 0\n\tglobal_load_lds_dwordx4 %1, off\n\ts_mov_b32 m0, %0" : "=&s"(keep) : "v"(g), "s"(lds_dst) : "memory");
}
DI void glds_c(const void* g, unsigned lds_dst) {
  unsigned keep;
  asm volatile("s_mov_b32 %0, m0\n\ts_mov_b32 m0, %2\n\ts_nop 0\n\tglobal_load_lds_dwordx4 %1, off sc1\n\ts_mov_b32 m0, %0" : "=&s"(keep) : "v"(g), "s"(lds_dst) : "memory");
}
DI int lane_id() { return __builtin_amdgcn_mbcnt_hi(~0u, __builtin_amdgcn_mbcnt_lo(~0u, 0u)); }
DI void barrier_raw() {
  asm volatile("s_waitcnt lgkmcnt(0)" ::: "memory");
  __builtin_amdgcn_s_barrier();
  asm volatile("" ::: "memory");
}
#define WAITV(n) asm volatile("s_waitcnt vmcnt(" #n ")" ::: "memory")
#define LGKM0() asm volatile("s_waitcnt lgkmcnt(0)" ::: "memory")
#define PIN_A(x) asm volatile("" : "+a"(x))
#define PIN_V(x) asm volatile("" : "+v"(x))
#define SCHED() __builtin_amdgcn_sched_barrier(0)
#define SMF(a, b, c) __builtin_amdgcn_smfmac_f32_16x16x64_bf16((a), (b), (c), sidx, 0, 0)
#define MF(a, b, c) __builtin_amdgcn_mfma_f32_16x16x32_bf16((a), (b), (c), 0, 0, 0)
#define MFV0(acc, a, b) asm volatile("v_mfma_f32_16x16x32_bf16 %0, %1, %2, 0" : "=&v"(acc) : "v"(a), "v"(b))
#define MFV(acc, a, b) asm volatile("v_mfma_f32_16x16x32_bf16 %0, %1, %2, %0" : "+v"(acc) : "v"(a), "v"(b))
DI float vmax(float a, float b) { float r; asm("v_max_f32 %0, %1, %2" : "=v"(r) : "v"(a), "v"(b)); return r; }
DI float vmax3(float a, float b, float c) { float r; asm("v_max3_f32 %0, %1, %2, %3" : "=v"(r) : "v"(a), "v"(b), "v"(c)); return r; }
DI rsrc_t mkrsrc(const void* p, unsigned bytes) { return __builtin_amdgcn_make_buffer_rsrc((void*)p, 0, bytes, 0x00020000); }
DI u32x4 ld128(rsrc_t r, unsigned v, unsigned s) { return __builtin_amdgcn_raw_buffer_load_b128(r, v, s, 16); }
DI u32x2 ld64(rsrc_t r, unsigned v, unsigned s) { return __builtin_amdgcn_raw_buffer_load_b64(r, v, s, 16); }
DI bf16x8 ldfrag(rsrc_t r, unsigned v, unsigned s) { return __builtin_bit_cast(bf16x8, ld128(r, v, s)); }
DI void st128(u32x4 x, rsrc_t r, unsigned v, unsigned s) { __builtin_amdgcn_raw_buffer_store_b128(x, r, v, s, 16); }
DI void st64(u32x2 x, rsrc_t r, unsigned v, unsigned s) { __builtin_amdgcn_raw_buffer_store_b64(x, r, v, s, 16); }
DI void st32(unsigned x, rsrc_t r, unsigned v, unsigned s) { __builtin_amdgcn_raw_buffer_store_b32(x, r, v, s, 16); }

#define CSTAMP(idx)
#define TREAD(var)
#define TSTORE(var, idx)
#define RSTAMP(idx)

struct Params {
  const u16* x; const u16* mem0; const u16* wq; const u16* wk; const u16* wv; const u16* wo;
  const u16* wg; const u16* wu; const u16* wd;
  u16* out; char* ws; char* ex;
};

DI float dpp_ror8(float x) { return __int_as_float(__builtin_amdgcn_update_dpp(0, __float_as_int(x), 0x128, 0xf, 0xf, false)); }
DI float rsum16(float x) {
  x += dpp_ror8(x);
  x += __int_as_float(__builtin_amdgcn_update_dpp(0, __float_as_int(x), 0x124, 0xf, 0xf, false));
  x += __int_as_float(__builtin_amdgcn_update_dpp(0, __float_as_int(x), 0x122, 0xf, 0xf, false));
  x += __int_as_float(__builtin_amdgcn_update_dpp(0, __float_as_int(x), 0x121, 0xf, 0xf, false));
  return x;
}
DI unsigned umax4(u32x4 v) { return max(max(v[0], v[1]), max(v[2], v[3])); }
DI unsigned nosent(unsigned v) { return min(v, 0xfffffffeu); }
DI void swap_add(const f32x4& a, const f32x4& b_in, f32x4& out) {
#pragma unroll
  for (int r = 0; r < 4; ++r) {
    auto sw = __builtin_amdgcn_permlane32_swap(__float_as_uint(a[r]), __float_as_uint(b_in[r]), false, false);
    out[r] = __uint_as_float(sw[0]) + __uint_as_float(sw[1]);
  }
}
DI float xmax16(float x) {
  auto a = __builtin_amdgcn_permlane16_swap(__float_as_uint(x), __float_as_uint(x), false, false);
  return vmax(__uint_as_float(a[0]), __uint_as_float(a[1]));
}
DI float xmax32(float x) {
  auto b = __builtin_amdgcn_permlane32_swap(__float_as_uint(x), __float_as_uint(x), false, false);
  return vmax(__uint_as_float(b[0]), __uint_as_float(b[1]));
}
DI float xsum16(float x) {
  auto a = __builtin_amdgcn_permlane16_swap(__float_as_uint(x), __float_as_uint(x), false, false);
  return __uint_as_float(a[0]) + __uint_as_float(a[1]);
}
DI float xsum32(float x) {
  auto b = __builtin_amdgcn_permlane32_swap(__float_as_uint(x), __float_as_uint(x), false, false);
  return __uint_as_float(b[0]) + __uint_as_float(b[1]);
}

constexpr unsigned SPIN_BUDGET = 1u << 21;
DI void wait_flags(rsrc_t rc, unsigned voff, unsigned soff, unsigned& budget) {
  for (;;) {
    asm volatile("" ::: "memory");
    unsigned v = __builtin_amdgcn_raw_buffer_load_b32(rc, voff, soff, 16);
    if (__all(v != 0xffffffffu) || budget == 0) break;
    --budget;
    __builtin_amdgcn_s_sleep(3);
  }
  asm volatile("" ::: "memory");
}
DI void set_flag(rsrc_t rc, unsigned soff) {
  st32(1u, rc, lane_id() == 0 ? 0u : OOB, soff);
}
DI unsigned wait_par(rsrc_t rc, unsigned& budget) {
  unsigned v;
  for (;;) {
    asm volatile("" ::: "memory");
    v = __builtin_amdgcn_readfirstlane(__builtin_amdgcn_raw_buffer_load_b32(rc, 0, F_PAR, 16));
    if (v != 0xffffffffu || budget == 0) break;
    --budget;
    __builtin_amdgcn_s_sleep(4);
  }
  return v & 1u;
}
DI void calibrate(const Params& p, char* lds, const int wave) {
  const int lane = lane_id();
  LAS unsigned* tm = (LAS unsigned*)lds;
  const rsrc_t r = mkrsrc(p.ex, EX_FLAGS);
  const rsrc_t rc = mkrsrc(p.ex + EX_FLAGS, F_END);
  st32(1u, r, exphys(EX_CAL + (wave * 16 + (lane & 15)) * 128, (lane >> 4) & 1), 0);
  asm volatile("s_waitcnt vmcnt(0)" ::: "memory");
  unsigned best0 = 0xffffffffu, best1 = 0xffffffffu;
  for (int rep = 0; rep < 16; ++rep) {
    unsigned off = exphys(EX_CAL + (wave * 16 + (rep >> 1)) * 128, rep & 1);
    long long t0 = __builtin_amdgcn_s_memtime();
    asm volatile("s_waitcnt lgkmcnt(0)" ::: "memory");
    unsigned x = __builtin_amdgcn_raw_buffer_load_b32(r, off, 0, 16);
    asm volatile("s_waitcnt vmcnt(0)" :: "v"(x) : "memory");
    long long t1 = __builtin_amdgcn_s_memtime();
    unsigned d = (unsigned)(t1 - t0);
    if (rep & 1) best1 = min(best1, d); else best0 = min(best0, d);
  }
  if (lane == 0) { tm[wave * 2] = best0; tm[wave * 2 + 1] = best1; }
  barrier_raw();
  if (wave == 0) {
    unsigned m0 = min(min(tm[0], tm[2]), min(tm[4], tm[6])), m1 = min(min(tm[1], tm[3]), min(tm[5], tm[7]));
    st32(m1 < m0 ? 1u : 0u, rc, lane == 0 ? 0u : OOB, F_PAR);
  }
  barrier_raw();
}

constexpr int L_XB = 0;
constexpr int L_QK = 8192;
constexpr int L_ST = 16384;

struct HeadCtx {
  LAS char* L;
  unsigned lbase;
  int lane, wave;
  unsigned qb[4];
};
DI bf16x16 frag4(LAS char* L, unsigned a0, unsigned a1, unsigned a2, unsigned a3) {
  s16x4 t0 = tr_read_l(L + a0), t1 = tr_read_l(L + a1), t2 = tr_read_l(L + a2), t3 = tr_read_l(L + a3);
  s16x8 a = __builtin_shufflevector(t0, t1, 0, 1, 2, 3, 4, 5, 6, 7);
  s16x8 b = __builtin_shufflevector(t2, t3, 0, 1, 2, 3, 4, 5, 6, 7);
  s16x16 c = __builtin_shufflevector(a, b, 0, 1, 2, 3, 4, 5, 6, 7, 8, 9, 10, 11, 12, 13, 14, 15);
  return __builtin_bit_cast(bf16x16, c);
}
DI void stage_half(const HeadCtx& c, const u16* W, int half) {
#pragma unroll 4
  for (int i = 0; i < 16; ++i) {
    int blk = half * 64 + c.wave * 16 + i;
    int row = blk * 4 + (c.lane >> 4);
    int ch = (c.lane & 15) ^ (((row & 3) << 2) | ((row >> 2) & 3));
    glds(W + (size_t)row * HD + ch * 8, c.lbase + L_ST + blk * 1024);
  }
}
DI bf16x16 proj_frag(const HeadCtx& c, int c2, int t) {
  unsigned add = ((t & 4) | (c.wave ^ (t & 3))) * 16384;
  asm volatile("" : "+s"(add));
  return frag4(c.L, (c.qb[0] ^ (c2 * 32)) + add, (c.qb[1] ^ (c2 * 32)) + add, (c.qb[2] ^ (c2 * 32)) + add, (c.qb[3] ^ (c2 * 32)) + add);
}
DI void head_ctx(HeadCtx& c, char* lds, int wave) {
  c.L = (LAS char*)lds;
  c.lbase = __builtin_amdgcn_readfirstlane((unsigned)(size_t)c.L);
  c.lane = lane_id(); c.wave = wave;
  const int gB = c.lane >> 4, q = (c.lane & 15) >> 2, pp = c.lane & 3;
#pragma unroll
  for (int u = 0; u < 4; ++u) {
    const int k = (u < 2) ? (8 * gB + 4 * u + q) : (32 + 8 * gB + 4 * (u - 2) + q);
    const int rq = 16 * (k >> 4) + 8 * ((k >> 1) & 1) + 2 * ((k >> 2) & 3) + (k & 1);
    c.qb[u] = L_ST + off_b(rq, 4 * wave + (pp >> 1)) + 8 * (pp & 1);
  }
}
#define XREAD(t) (*(const LAS bf16x8*)(xa[(t) & 3] + L_XB + ((t) & 4) * 1024))
#define AFLOAD(t) af[t] = XREAD(t)
#define PSTEP(w, a0, a1, t) do { a0 = SMF(af[t], w[0][t], a0); a1 = SMF(af[t], w[1][t], a1); asm volatile("" : "+v"(a0), "+v"(a1)); } while (0)
#define GATHER(kind)                                                                                            \
  {                                                                                                             \
    u32x4 f0, f1;                                                                                               \
    if (s == 0) {                                                                                               \
      const char* m0 = (const char*)p.mem0 + (r16 & 7) * 1024 + g * 32 + (r16 >> 3) * 16 + wave * 128;          \
      f0 = *(const u32x4*)m0; f1 = *(const u32x4*)(m0 + 512);                                                   \
    } else {                                                                                                    \
      const unsigned so = exphys(EX_MEM + (s & 3) * 8192, par) + wave * 1024;                                   \
      for (;;) {                                                                                                \
        asm volatile("" ::: "memory");                                                                          \
        f0 = ld128(rex, lane16, so);                                                                            \
        f1 = ld128(rex, lane16, so + 4096);                                                                     \
        unsigned mxv = max(umax4(f0), umax4(f1));                                                               \
        if (__all(mxv != 0xffffffffu) || budget == 0) break;                                                    \
        --budget;                                                                                               \
      }                                                                                                         \
    }                                                                                                           \
    TREAD(t_arr);                                                                                               \
    *(LAS u32x4*)(xa[0] + L_XB) = f0;                                                                           \
    *(LAS u32x4*)(xa[0] + L_XB + 4096) = f1;                                                                    \
    af[0] = __builtin_bit_cast(bf16x8, f0);                                                                     \
    af[4] = __builtin_bit_cast(bf16x8, f1);                                                                     \
  }

DI void a_head(const Params& p, int h, char* lds, const int wave) {
  HeadCtx c; head_ctx(c, lds, wave);
  const int lane = c.lane, g = lane >> 4, r16 = lane & 15;
  LAS char* L = c.L;
  const rsrc_t rex = mkrsrc(p.ex, EX_FLAGS);
  const rsrc_t rc = mkrsrc(p.ex + EX_FLAGS, F_END);
  const int sidx = r16 < 8 ? 0x4444 : 0xEEEE;
  unsigned budget = SPIN_BUDGET;
  bf16x16 wq[2][8], wk[2][8];
  stage_half(c, p.wq + h * 128, 0); stage_half(c, p.wq + h * 128, 1);
  WAITV(16); barrier_raw();
#pragma unroll
  for (int c2 = 0; c2 < 2; ++c2)
#pragma unroll
    for (int t = 0; t < 4; ++t) { wq[c2][t] = proj_frag(c, c2, t); PIN_A(wq[c2][t]); }
  barrier_raw();
  stage_half(c, p.wk + h * 128, 0);
  WAITV(16); barrier_raw();
#pragma unroll
  for (int c2 = 0; c2 < 2; ++c2)
#pragma unroll
    for (int t = 4; t < 8; ++t) { wq[c2][t] = proj_frag(c, c2, t); PIN_A(wq[c2][t]); }
  barrier_raw();
  stage_half(c, p.wk + h * 128, 1);
  WAITV(16); barrier_raw();
#pragma unroll
  for (int c2 = 0; c2 < 2; ++c2)
#pragma unroll
    for (int t = 0; t < 4; ++t) { wk[c2][t] = proj_frag(c, c2, t); PIN_A(wk[c2][t]); }
  WAITV(0); barrier_raw();
#pragma unroll
  for (int c2 = 0; c2 < 2; ++c2)
#pragma unroll
    for (int t = 4; t < 8; ++t) { wk[c2][t] = proj_frag(c, c2, t); PIN_A(wk[c2][t]); }

  const rsrc_t rq = mkrsrc(p.ws + WS_Q, 32 * MB);
  const rsrc_t rpt = mkrsrc(p.ws + WS_PT, 4 * MB);
  const unsigned par = wait_par(rc, budget);
  wait_flags(rc, (lane & 3) * 4, F_QK + h * 16, budget);
  const float sc = 0.08838834764831845f * 1.4426950408889634f;
  const unsigned lane16 = lane * 16;
  LAS char* xa[4];
#pragma unroll
  for (int j = 0; j < 4; ++j) xa[j] = L + ((lane16 + wave * 1024) ^ (j * 1024));
  LAS char* qk_w = L + L_QK + (wave * 32 + (lane >> 5) * 16 + r16) * 32 + (g & 1) * 8;
  LAS char* qk_r = L + L_QK + (g * 8 + (r16 >> 2)) * 32 + (lane & 1) * 8;
  if (h == 0) RSTAMP(201);

  auto run = [&](auto role_c) {
    constexpr int ROLE = decltype(role_c)::v;
    bf16x8 k1[4], k2[4], qf[4];
    f32x4 s1, s2;
    const float NEG = -3.0e38f;
    auto prefetch = [&](int s) {
      if constexpr (ROLE < 2) {
        const unsigned so = (unsigned)(s * NH + h) * 8192;
#pragma unroll
        for (int kk = 0; kk < 4; ++kk) {
          k1[kk] = ldfrag(rq, lane16 + kk * 1024, so + (unsigned)(16 * MB));
          k2[kk] = ldfrag(rq, lane16 + kk * 1024, so + (unsigned)(16 * MB) + 4096);
        }
        if constexpr (ROLE == 1) {
#pragma unroll
          for (int kk = 0; kk < 4; ++kk) qf[kk] = ldfrag(rq, lane16 + kk * 1024, so + (wave - 1) * 4096);
        }
      }
    };
    auto tok_scores = [&]() {
      MFV0(s1, k1[0], qf[0]); MFV0(s2, k2[0], qf[0]);
#pragma unroll
      for (int kk = 1; kk < 4; ++kk) { MFV(s1, k1[kk], qf[kk]); MFV(s2, k2[kk], qf[kk]); }
      asm volatile("s_nop 7\n\ts_nop 1" : "+v"(s1), "+v"(s2));
      const int lim1 = wave == 1 ? r16 : 64, lim2 = wave == 1 ? -1 : r16;
#pragma unroll
      for (int r = 0; r < 4; ++r) {
        if (g * 4 + r > lim1) s1[r] = NEG;
        if (g * 4 + r > lim2) s2[r] = NEG;
      }
      asm volatile("" : "+v"(s1), "+v"(s2));
    };
    prefetch(0);
    if constexpr (ROLE == 1) tok_scores();
    const unsigned vo_pt = ROLE == 0 ? (unsigned)(g * 128 + (r16 & 7) * 16 + (r16 >> 3) * 8) : 0u;
    const unsigned vo_pm = ROLE == 0 ? (g < 2 ? (unsigned)(512 + (r16 & 7) * 16 + g * 8 + (r16 >> 3) * 4) : OOB)
                                     : (g < 2 ? (unsigned)(1536 + (wave - 1) * 256 + r16 * 16 + g * 8) : OOB);
    const bool hi = r16 >= 8;

    for (int s = 0; s < NSEG; ++s) {
      bf16x8 af[8];
      f32x4 z4 = {0.f, 0.f, 0.f, 0.f};
      f32x4 aq0 = z4, aq1 = z4, ak0 = z4, ak1 = z4;
      asm volatile("" : "+v"(aq0), "+v"(aq1), "+v"(ak0), "+v"(ak1));
      long long t_arr = 0;
      GATHER(0);
      SCHED();
      PSTEP(wq, aq0, aq1, 0); PSTEP(wq, aq0, aq1, 4);
      barrier_raw();
      AFLOAD(1); AFLOAD(2); AFLOAD(3); AFLOAD(5); AFLOAD(6); AFLOAD(7);
      SCHED();
      PSTEP(wk, ak0, ak1, 0); PSTEP(wk, ak0, ak1, 4);
      SCHED();
      PSTEP(wq, aq0, aq1, 1); PSTEP(wq, aq0, aq1, 2); PSTEP(wq, aq0, aq1, 3);
      PSTEP(wq, aq0, aq1, 5); PSTEP(wq, aq0, aq1, 6); PSTEP(wq, aq0, aq1, 7);
      PSTEP(wk, ak0, ak1, 1);
      SCHED();
      {
        f32x4 sq;
        swap_add(aq0, aq1, sq);
        u32x2 vq = {pack2(sq[0], sq[1]), pack2(sq[2], sq[3])};
        *(LAS u32x2*)(qk_w) = vq;
      }
      PSTEP(wk, ak0, ak1, 2);
      SCHED();
      barrier_raw();
      if constexpr (ROLE == 0) {
#pragma unroll
        for (int kk = 0; kk < 4; ++kk) qf[kk] = tr_frag(qk_r + kk * 1024, qk_r + kk * 1024 + 128);
      }
      if constexpr (ROLE == 2) {
        const u32x4 sent = {0xffffffffu, 0xffffffffu, 0xffffffffu, 0xffffffffu};
        st128(sent, rex, lane < 40 ? lane16 : OOB, exphys(EX_P + (unsigned)((s + 3) & 3) * 32768 + h * 1024, par));
      }
      SCHED();
      PSTEP(wk, ak0, ak1, 3); PSTEP(wk, ak0, ak1, 5); PSTEP(wk, ak0, ak1, 6); PSTEP(wk, ak0, ak1, 7);
      SCHED();
      if constexpr (ROLE == 0) {
        MFV0(s1, k1[0], qf[0]); MFV0(s2, k2[0], qf[0]);
        SCHED();
      }
      {
        f32x4 sk;
        swap_add(ak0, ak1, sk);
        u32x2 vk = {pack2(sk[0], sk[1]), pack2(sk[2], sk[3])};
        *(LAS u32x2*)(qk_w + 16) = vk;
      }
      SCHED();
      if constexpr (ROLE == 0) {
        MFV(s1, k1[1], qf[1]); MFV(s2, k2[1], qf[1]);
        SCHED();
      }
      barrier_raw();
      if constexpr (ROLE == 0) {
        if (s < NSEG - 1) {
          bf16x8 kf[4];
#pragma unroll
          for (int kk = 0; kk < 4; ++kk) kf[kk] = tr_frag(qk_r + kk * 1024 + 16, qk_r + kk * 1024 + 144);
          const unsigned so = exphys(EX_P + (unsigned)(s & 3) * 32768 + h * 1024, par);
          SCHED();
          f32x4 sm;
          MFV(s1, k1[2], qf[2]); MFV(s2, k2[2], qf[2]);
          MFV(s1, k1[3], qf[3]); MFV(s2, k2[3], qf[3]);
          MFV0(sm, kf[0], qf[0]); MFV(sm, kf[1], qf[1]);
          asm volatile("s_nop 1" : "+v"(s1), "+v"(s2));
          f32x4 t;
#pragma unroll
          for (int r = 0; r < 4; ++r) t[r] = hi ? s2[r] : s1[r];
          SCHED();
          MFV(sm, kf[2], qf[2]);
          float mt = vmax3(vmax(t[0], t[1]), t[2], t[3]);
          mt = vmax(mt, dpp_ror8(mt));
          SCHED();
          MFV(sm, kf[3], qf[3]);
          mt = xmax32(xmax16(mt));
          asm volatile("s_nop 1" : "+v"(sm), "+v"(mt));
          const float mx = xmax16(vmax3(vmax3(sm[0], sm[1], sm[2]), sm[3], mt));
          const float m0 = hi ? sm[2] : sm[0], m1 = hi ? sm[3] : sm[1];
          const float nm = -mx * sc;
#pragma unroll
          for (int r = 0; r < 4; ++r) t[r] = __builtin_amdgcn_exp2f(t[r] * sc + nm);
          const float a0 = __builtin_amdgcn_exp2f(m0 * sc + nm), a1 = __builtin_amdgcn_exp2f(m1 * sc + nm);
          float l = (a0 + a1) * 0.5f + ((t[0] + t[1]) + (t[2] + t[3]));
          l += dpp_ror8(l);
          l = xsum32(xsum16(l));
          const float inv = __builtin_amdgcn_rcpf(l);
          u32x2 pt = {nosent(pack2(t[0] * inv, t[1] * inv)), nosent(pack2(t[2] * inv, t[3] * inv))};
          const unsigned pm = nosent(pack2(a0 * inv, a1 * inv));
          st64(pt, rex, vo_pt, so);
          st32(pm, rex, vo_pm, so);
          SCHED();
          CSTAMP((1 * 64 + s) * 128 + h * 4);
          if (h == 0) RSTAMP(s);
          prefetch(s + 1);
        }
      } else if constexpr (ROLE == 1) {
        bf16x8 kf[4];
#pragma unroll
        for (int kk = 0; kk < 4; ++kk) kf[kk] = tr_frag(qk_r + kk * 1024 + 16, qk_r + kk * 1024 + 144);
        f32x4 sm;
        MFV0(sm, kf[0], qf[0]); MFV(sm, kf[1], qf[1]); MFV(sm, kf[2], qf[2]); MFV(sm, kf[3], qf[3]);
        float mx = vmax3(vmax3(s1[0], s1[1], s1[2]), vmax3(s1[3], s2[0], s2[1]), vmax(s2[2], s2[3]));
        asm volatile("s_nop 7\n\ts_nop 1" : "+v"(sm), "+v"(mx));
        mx = vmax3(vmax3(sm[0], sm[1], sm[2]), sm[3], mx);
        mx = xmax32(xmax16(mx));
        const float nm = -mx * sc;
        f32x4 a, b, cc;
#pragma unroll
        for (int r = 0; r < 4; ++r) {
          a[r] = __builtin_amdgcn_exp2f(s1[r] * sc + nm);
          b[r] = __builtin_amdgcn_exp2f(s2[r] * sc + nm);
          cc[r] = __builtin_amdgcn_exp2f(sm[r] * sc + nm);
        }
        float ls = ((a[0] + a[1]) + (a[2] + a[3])) + ((b[0] + b[1]) + (b[2] + b[3])) + (g < 2 ? ((cc[0] + cc[1]) + (cc[2] + cc[3])) : 0.f);
        ls = xsum32(xsum16(ls));
        const float inv = __builtin_amdgcn_rcpf(ls);
        u32x4 ptk = {pack2(a[0] * inv, a[1] * inv), pack2(a[2] * inv, a[3] * inv), pack2(b[0] * inv, b[1] * inv), pack2(b[2] * inv, b[3] * inv)};
        u32x2 pm = {pack2(cc[0] * inv, cc[1] * inv), pack2(cc[2] * inv, cc[3] * inv)};
        const unsigned so = (unsigned)(s * NH + h) * 2048;
        if (wave == 1) { u32x2 lo = {ptk[0], ptk[1]}; st64(lo, rpt, lane * 8, so); }
        else st128(ptk, rpt, lane16 + 512, so);
        st64(pm, rpt, vo_pm, so);
        SCHED();
        if (s + 1 < NSEG) prefetch(s + 1);
        WAITV(0);
        set_flag(rc, F_PT + (unsigned)(s * NH + h) * 8 + (wave - 1) * 4);
        if (s + 1 < NSEG) tok_scores();
      } else {
        if (!(s & 1) && s + 2 < NSEG) wait_flags(rc, (lane & 3) * 4, F_QK + (unsigned)(((s + 2) >> 1) * NH + h) * 16, budget);
      }
      TSTORE(t_arr, (0 * 64 + s) * 128 + h * 4 + wave);
    }
  };
  if (wave == 0) run(IC<0>{});
  else if (wave < 3) run(IC<1>{});
  else run(IC<2>{});
}

DI void v_head(const Params& p, int h, char* lds, const int wave) {
  HeadCtx c; head_ctx(c, lds, wave);
  const int lane = c.lane, g = lane >> 4, r16 = lane & 15;
  LAS char* L = c.L;
  const rsrc_t rex = mkrsrc(p.ex, EX_FLAGS);
  const rsrc_t rc = mkrsrc(p.ex + EX_FLAGS, F_END);
  const int sidx = r16 < 8 ? 0x4444 : 0xEEEE;
  unsigned budget = SPIN_BUDGET;
  bf16x16 wv[2][8], wo[4][4];
  unsigned ob[4];
  {
    const int gB = lane >> 4, q = (lane & 15) >> 2, pp = lane & 3;
#pragma unroll
    for (int u = 0; u < 4; ++u) {
      const int k = (u < 2) ? (8 * gB + 4 * u + q) : (32 + 8 * gB + 4 * (u - 2) + q);
      const int ro = 8 * (k >> 4) + 2 * ((k >> 2) & 3) + (k & 1);
      ob[u] = L_ST + wave * 8192 + off_b(ro, 2 * ((k >> 1) & 1) + (pp >> 1)) + 8 * (pp & 1);
    }
  }
  auto stage_wo = [&](int j) {
#pragma unroll
    for (int i = 0; i < 8; ++i) {
      int row = i * 4 + (lane >> 4);
      int ch = (lane & 15) ^ (((row & 3) << 2) | ((row >> 2) & 3));
      glds(p.wo + ((size_t)h * 128 + (wave ^ j) * 32 + row) * DM + wave * 128 + ch * 8, c.lbase + L_ST + j * 32768 + wave * 8192 + i * 1024);
    }
  };
  auto wo_frag = [&](int cp, int j) {
    unsigned add = j * 32768;
    asm volatile("" : "+s"(add));
    return frag4(L, (ob[0] ^ (cp * 64)) + add, (ob[1] ^ (cp * 64)) + add, (ob[2] ^ (cp * 64)) + add, (ob[3] ^ (cp * 64)) + add);
  };
  stage_half(c, p.wv + h * 128, 0); stage_half(c, p.wv + h * 128, 1);
  WAITV(16); barrier_raw();
#pragma unroll
  for (int c2 = 0; c2 < 2; ++c2)
#pragma unroll
    for (int t = 0; t < 4; ++t) { wv[c2][t] = proj_frag(c, c2, t); PIN_A(wv[c2][t]); }
  barrier_raw();
  stage_wo(0); stage_wo(1);
  WAITV(16); barrier_raw();
#pragma unroll
  for (int c2 = 0; c2 < 2; ++c2)
#pragma unroll
    for (int t = 4; t < 8; ++t) { wv[c2][t] = proj_frag(c, c2, t); PIN_A(wv[c2][t]); }
  barrier_raw();
  stage_wo(2); stage_wo(3);
  WAITV(16);
#pragma unroll
  for (int j = 0; j < 2; ++j)
#pragma unroll
    for (int cp = 0; cp < 4; ++cp) { wo[cp][j] = wo_frag(cp, j); PIN_A(wo[cp][j]); }
  WAITV(0);
#pragma unroll
  for (int j = 2; j < 4; ++j)
#pragma unroll
    for (int cp = 0; cp < 4; ++cp) { wo[cp][j] = wo_frag(cp, j); PIN_A(wo[cp][j]); }

  const rsrc_t rvo = mkrsrc(p.ws + WS_VO, 16 * MB);
  const unsigned par = wait_par(rc, budget);
  const unsigned lane16 = lane * 16;
  LAS char* xa[4];
  LAS char* vr[4];
#pragma unroll
  for (int j = 0; j < 4; ++j) {
    xa[j] = L + ((lane16 + wave * 1024) ^ (j * 1024));
    vr[j] = L + L_QK + (((g * 8 + (r16 >> 2)) * 32 + (lane & 1) * 8 + wave * 1024) ^ (j * 1024));
  }
  LAS char* qk_w = L + L_QK + (wave * 32 + (lane >> 5) * 16 + r16) * 32 + (g & 1) * 8;
  const unsigned part_off = (unsigned)((wave * 128 + (g >> 1) * 16 + r16) * 16 + (g & 1) * 8);

  for (int s = 0; s < NSEG; ++s) {
    bf16x8 af[8];
    f32x4 z4 = {0.f, 0.f, 0.f, 0.f};
    f32x4 av0 = z4, av1 = z4;
    f32x4 ap[4] = {z4, z4, z4, z4};
    asm volatile("" : "+v"(av0), "+v"(av1), "+v"(ap[0]), "+v"(ap[1]), "+v"(ap[2]), "+v"(ap[3]));
    long long t_arr = 0;
    GATHER(4);
    SCHED();
    PSTEP(wv, av0, av1, 0);
    barrier_raw();
    AFLOAD(1); AFLOAD(2); AFLOAD(3); AFLOAD(5); AFLOAD(6); AFLOAD(7);
    SCHED();
    PSTEP(wv, av0, av1, 4);
    SCHED();
    PSTEP(wv, av0, av1, 1); PSTEP(wv, av0, av1, 2); PSTEP(wv, av0, av1, 3);
    PSTEP(wv, av0, av1, 5); PSTEP(wv, av0, av1, 6); PSTEP(wv, av0, av1, 7);
    SCHED();
    bf16x8 of[4];
    {
      f32x4 sv;
      swap_add(av0, av1, sv);
      u32x2 vv = {pack2(sv[0], sv[1]), pack2(sv[2], sv[3])};
      *(LAS u32x2*)(qk_w) = vv;
    }
    asm volatile("" ::: "memory");
    of[0] = tr_frag(vr[0], vr[0] + 128);
    barrier_raw();
#pragma unroll
    for (int j = 1; j < 4; ++j) of[j] = tr_frag(vr[j], vr[j] + 128);
    SCHED();
#pragma unroll
    for (int j = 0; j < 4; ++j)
#pragma unroll
      for (int cp = 0; cp < 4; ++cp) ap[cp] = SMF(of[j], wo[cp][j], ap[cp]);
    u32x2 v[4];
#pragma unroll
    for (int cp = 0; cp < 4; ++cp) { v[cp][0] = nosent(pack2(ap[cp][0], ap[cp][1])); v[cp][1] = nosent(pack2(ap[cp][2], ap[cp][3])); }
    if (s < NSEG - 1) {
      const unsigned so = exphys(EX_VO + (unsigned)((s & 1) * 32 + h) * 8192, par);
#pragma unroll
      for (int cp = 0; cp < 4; ++cp) st64(v[cp], rex, part_off + cp * 512, so);
    }
    SCHED();
    CSTAMP((5 * 64 + s) * 128 + h * 4 + wave);
    TSTORE(t_arr, (4 * 64 + s) * 128 + h * 4 + wave);
#pragma unroll
    for (int cp = 0; cp < 4; ++cp) st64(v[cp], rvo, part_off + cp * 512, (unsigned)(s * NH + h) * 8192);
    WAITV(0);
    set_flag(rc, F_VO + (unsigned)((s * 4 + wave) * 32 + h) * 4);
  }
}

DI void reducer(const Params& p, int ct, char* lds, const int wave) {
  const int lane = lane_id(), g = lane >> 4, r16 = lane & 15;
  LAS char* L = (LAS char*)lds;
  const rsrc_t rex = mkrsrc(p.ex, EX_FLAGS);
  const rsrc_t rc = mkrsrc(p.ex + EX_FLAGS, F_END);
  const rsrc_t rvw = mkrsrc(p.ws + WS_VW, 64 * MB);
  unsigned budget = SPIN_BUDGET * 2;
  const unsigned lane16 = lane * 16;
  const unsigned vo_p = (unsigned)((r16 >> 3) * 1024 + g * 128 + (r16 & 7) * 16);
  const unsigned vo_m = (unsigned)((4 * (r16 >> 3) + g) * 1024 + 512 + (r16 & 7) * 16);
  const unsigned vo_v = (unsigned)(g * 16384 + (ct * 16 + r16) * 16);
  const unsigned vo_o = r16 < 8 ? (unsigned)((2 * ct + (g >> 1)) * 128 + r16 * 16 + (g & 1) * 8) : OOB;
  const bool hi = r16 >= 8;
  const unsigned par = wait_par(rc, budget);
  bf16x8 vw[2][8];
  auto prefetch = [&](auto bc, int s) {
    constexpr int B = decltype(bc)::v;
    if (!(s & 1)) wait_flags(rc, (lane & 7) * 4, F_VW + (unsigned)(((s >> 1) * 4 + (ct >> 3)) * 32 + wave * 8) * 4, budget);
    const unsigned so = (unsigned)((s * 32 + ct) * 32 + wave * 8) * 1024;
#pragma unroll
    for (int j = 0; j < 4; ++j) { vw[B][j] = ldfrag(rvw, lane16 + j * 1024, so); vw[B][4 + j] = ldfrag(rvw, lane16 + j * 1024, so + 4096); }
  };
  auto step = [&](auto bc, int s) {
    constexpr int B = decltype(bc)::v;
    u32x4 pt[4], pm, vo[2];
    const unsigned sp = exphys(EX_P + (unsigned)(s & 3) * 32768 + wave * 8192, par);
    const unsigned sv = exphys(EX_VO + (unsigned)(B * 32 + wave * 8) * 8192, par);
    for (;;) {
      asm volatile("" ::: "memory");
      vo[0] = ld128(rex, vo_v, sv);
      vo[1] = ld128(rex, vo_v, sv + 65536);
      pt[0] = ld128(rex, vo_p, sp);
      pt[1] = ld128(rex, vo_p + 2048, sp);
      pt[2] = ld128(rex, vo_p, sp + 4096);
      pt[3] = ld128(rex, vo_p + 2048, sp + 4096);
      pm = ld128(rex, vo_m, sp);
      unsigned mxv = max(max(max(umax4(vo[0]), umax4(vo[1])), max(umax4(pt[0]), umax4(pt[1]))), max(max(umax4(pt[2]), umax4(pt[3])), umax4(pm)));
      if (__all(mxv != 0xffffffffu) || budget == 0) break;
      --budget;
    }
    long long t_arr = 0;
    TREAD(t_arr);
    f32x4 a0, a1;
    MFV0(a0, vo[0], pm); MFV0(a1, vo[1], pm);
#pragma unroll
    for (int i = 0; i < 4; ++i) { MFV(a0, vw[B][2 * i], pt[i]); MFV(a1, vw[B][2 * i + 1], pt[i]); }
    asm volatile("s_nop 7" : "+v"(a0), "+v"(a1));
    f32x4 t;
#pragma unroll
    for (int r = 0; r < 4; ++r) t[r] = hi ? a1[r] : a0[r];
    if (wave != 0) *(LAS f32x4*)(L + wave * 1024 + lane16) = t;
    barrier_raw();
    const u32x4 sent = {0xffffffffu, 0xffffffffu, 0xffffffffu, 0xffffffffu};
    if (wave == 0) {
      f32x4 u1 = *(const LAS f32x4*)(L + 1024 + lane16), u2 = *(const LAS f32x4*)(L + 2048 + lane16), u3 = *(const LAS f32x4*)(L + 3072 + lane16);
      t = (t + u1) + (u2 + u3);
#pragma unroll
      for (int r = 0; r < 4; ++r) t[r] += dpp_ror8(t[r]);
      u32x2 ob = {nosent(pack2(t[0], t[1])), nosent(pack2(t[2], t[3]))};
      st64(ob, rex, vo_o, exphys(EX_MEM + (unsigned)((s + 1) & 3) * 8192, par));
      SCHED();
      CSTAMP((3 * 64 + s) * 128 + ct);
      u32x2 s2 = {0xffffffffu, 0xffffffffu};
      st64(s2, rex, vo_o, exphys(EX_MEM + (unsigned)(s & 3) * 8192, par));
    }
    st128(sent, rex, vo_v, sv);
    st128(sent, rex, vo_v, sv + 65536);
    TSTORE(t_arr, (2 * 64 + s) * 128 + ct * 4 + wave);
    if (s + 2 < NSEG - 1) prefetch(bc, s + 2);
  };
  prefetch(IC<0>{}, 0);
  prefetch(IC<1>{}, 1);
  for (int s = 0; s < NSEG - 2; s += 2) { step(IC<0>{}, s); step(IC<1>{}, s + 1); }
  step(IC<0>{}, NSEG - 2);
}

DI void load_proj(const u16* W, LAS char* L, unsigned lbase, int lane, int wave, bf16x8 (&wf)[2][16]) {
  const int g = lane >> 4, r16 = lane & 15;
#pragma unroll 4
  for (int i = 0; i < 32; ++i) {
    int blk = wave * 32 + i;
    int row = blk * 4 + (lane >> 4);
    int ch = (lane & 15) ^ (((row & 3) << 2) | ((row >> 2) & 3));
    glds(W + (size_t)row * HD + ch * 8, lbase + blk * 1024);
  }
  WAITV(0);
  barrier_raw();
  const int q = r16 >> 2, pp = lane & 3;
#pragma unroll
  for (int t = 0; t < 2; ++t) {
    const unsigned o0 = off_b(8 * g + q, 4 * wave + pp) + 8 * t, o1 = off_b(8 * g + 4 + q, 4 * wave + pp) + 8 * t;
#pragma unroll
    for (int kk = 0; kk < 16; ++kk) wf[t][kk] = tr_frag(L + o0 + kk * 8192, L + o1 + kk * 8192);
  }
  barrier_raw();
}
DI void stage_xn(const Params& p, unsigned lbase, int lane, int wave, int tile) {
  const u16* xn = (const u16*)(p.ws + WS_XN);
#pragma unroll 4
  for (int i = 0; i < 16; ++i) {
    int row = wave * 16 + i;
    glds_c(xn + ((size_t)tile * 64 + row) * DM + ((lane ^ (row & 15)) * 8), lbase + (tile & 1) * 65536 + row * 1024);
  }
}

DI void qk_worker(const Params& p, int h, char* lds, const int wave) {
  const int lane = lane_id(), g = lane >> 4, r16 = lane & 15;
  LAS char* L = (LAS char*)lds;
  const unsigned lbase = __builtin_amdgcn_readfirstlane((unsigned)(size_t)L);
  const rsrc_t rc = mkrsrc(p.ex + EX_FLAGS, F_END);
  unsigned budget = SPIN_BUDGET;
  bf16x8 wf[2][2][16];
  load_proj(p.wk + h * 128, L, lbase, lane, wave, wf[1]);
  load_proj(p.wq + h * 128, L, lbase, lane, wave, wf[0]);
  const rsrc_t ro = mkrsrc(p.ws + WS_Q, 32 * MB);
  wait_flags(rc, (lane & 31) * 4, F_XN, budget);
  stage_xn(p, lbase, lane, wave, 0);
  for (int tile = 0; tile < 32; ++tile) {
    if (tile < 2) WAITV(0); else WAITV(8);
    barrier_raw();
    if (h == 0) RSTAMP(64 + tile);
    if (tile + 1 < 32) stage_xn(p, lbase, lane, wave, tile + 1);
    LAS char* A = L + (tile & 1) * 65536 + r16 * 1024;
    f32x4 acc[2][4][2] = {};
    bf16x8 xf[16][4];
#pragma unroll
    for (int kk = 0; kk < 3; ++kk)
#pragma unroll
      for (int m = 0; m < 4; ++m) xf[kk][m] = *(const LAS bf16x8*)(A + m * 16384 + (((4 * kk + g) ^ r16) * 16));
    SCHED();
#pragma unroll
    for (int kk = 0; kk < 16; ++kk) {
      if (kk + 3 < 16) {
#pragma unroll
        for (int m = 0; m < 4; ++m) xf[kk + 3][m] = *(const LAS bf16x8*)(A + m * 16384 + (((4 * (kk + 3) + g) ^ r16) * 16));
      }
#pragma unroll
      for (int m = 0; m < 4; ++m)
#pragma unroll
        for (int sl = 0; sl < 2; ++sl) {
          acc[sl][m][0] = MF(wf[sl][0][kk], xf[kk][m], acc[sl][m][0]);
          acc[sl][m][1] = MF(wf[sl][1][kk], xf[kk][m], acc[sl][m][1]);
        }
      if (kk == 9 && tile > 1) {
        if (tile + 1 < 32) WAITV(16); else WAITV(0);
        set_flag(rc, F_QK + (unsigned)((tile - 1) * NH + h) * 16 + wave * 4);
      }
      SCHED();
    }
#pragma unroll
    for (int sl = 0; sl < 2; ++sl)
#pragma unroll
      for (int m = 0; m < 4; ++m) {
        const f32x4 &a = acc[sl][m][0], &b = acc[sl][m][1];
        u32x4 v = {pack2(a[0], a[1]), pack2(a[2], a[3]), pack2(b[0], b[1]), pack2(b[2], b[3])};
        st128(v, ro, lane * 16, (unsigned)(sl * 16 * MB) + (unsigned)((tile * 2 + (m >> 1)) * NH + h) * 8192 + ((m & 1) * 4 + wave) * 1024);
      }
    if (tile == 0 || tile == 31) {
      WAITV(0);
      set_flag(rc, F_QK + (unsigned)(tile * NH + h) * 16 + wave * 4);
    }
  }
}

DI void vw_worker(const Params& p, int h, char* lds, const int wave) {
  const int lane = lane_id(), g = lane >> 4, r16 = lane & 15;
  LAS char* L = (LAS char*)lds;
  const unsigned lbase = __builtin_amdgcn_readfirstlane((unsigned)(size_t)L);
  const rsrc_t rc = mkrsrc(p.ex + EX_FLAGS, F_END);
  unsigned budget = SPIN_BUDGET;
  bf16x8 wf[2][16], wo[8][4];
  load_proj(p.wv + h * 128, L, lbase, lane, wave, wf);
  {
#pragma unroll 4
    for (int i = 0; i < 32; ++i) {
      int row = i * 4 + (lane >> 4);
      int ch = (lane & 15) ^ (((row & 3) << 2) | ((row >> 2) & 3));
      glds(p.wo + ((size_t)h * 128 + row) * DM + wave * 128 + ch * 8, lbase + wave * 32768 + i * 1024);
    }
    WAITV(0);
    const int q = r16 >> 2, pp = lane & 3;
#pragma unroll
    for (int n = 0; n < 8; ++n) {
      const unsigned o0 = wave * 32768 + off_b(8 * g + q, 2 * n + (pp >> 1)) + 8 * (pp & 1);
      const unsigned o1 = wave * 32768 + off_b(8 * g + 4 + q, 2 * n + (pp >> 1)) + 8 * (pp & 1);
#pragma unroll
      for (int kk = 0; kk < 4; ++kk) wo[n][kk] = tr_frag(L + o0 + kk * 8192, L + o1 + kk * 8192);
    }
    barrier_raw();
  }
  const rsrc_t ro = mkrsrc(p.ws + WS_VW, 64 * MB);
  LAS char* VX = L + 131072 + lane * 16;
  wait_flags(rc, (lane & 31) * 4, F_XN, budget);
  stage_xn(p, lbase, lane, wave, 0);
  for (int tile = 0; tile < 32; ++tile) {
    if (tile < 2) WAITV(0); else WAITV(16);
    barrier_raw();
    if (h == 0) RSTAMP(96 + tile);
    if (tile + 1 < 32) stage_xn(p, lbase, lane, wave, tile + 1);
    LAS char* A = L + (tile & 1) * 65536 + r16 * 1024;
    {
      f32x4 acc[4][2] = {};
      bf16x8 xf[16][4];
#pragma unroll
      for (int kk = 0; kk < 4; ++kk)
#pragma unroll
        for (int m = 0; m < 4; ++m) xf[kk][m] = *(const LAS bf16x8*)(A + m * 16384 + (((4 * kk + g) ^ r16) * 16));
      SCHED();
#pragma unroll
      for (int kk = 0; kk < 16; ++kk) {
        if (kk + 4 < 16) {
#pragma unroll
          for (int m = 0; m < 4; ++m) xf[kk + 4][m] = *(const LAS bf16x8*)(A + m * 16384 + (((4 * (kk + 4) + g) ^ r16) * 16));
        }
#pragma unroll
        for (int m = 0; m < 4; ++m) { acc[m][0] = MF(wf[0][kk], xf[kk][m], acc[m][0]); acc[m][1] = MF(wf[1][kk], xf[kk][m], acc[m][1]); }
        SCHED();
      }
#pragma unroll
      for (int m = 0; m < 4; ++m) {
        const f32x4 &a = acc[m][0], &b = acc[m][1];
        u32x4 v = {pack2(a[0], a[1]), pack2(a[2], a[3]), pack2(b[0], b[1]), pack2(b[2], b[3])};
        *(LAS u32x4*)(VX + (m * 4 + wave) * 1024) = v;
      }
    }
    if (tile > 1) {
      if (tile + 1 < 32) WAITV(16); else WAITV(0);
      set_flag(rc, F_VW + (unsigned)(((tile - 1) * 4 + wave) * 32 + h) * 4);
    }
    barrier_raw();
    bf16x8 vf[4][4];
#pragma unroll
    for (int m = 0; m < 4; ++m)
#pragma unroll
      for (int kk = 0; kk < 4; ++kk) vf[m][kk] = *(const LAS bf16x8*)(VX + (m * 4 + kk) * 1024);
    SCHED();
#pragma unroll
    for (int n = 0; n < 8; ++n) {
      f32x4 a2[4] = {};
#pragma unroll
      for (int kk = 0; kk < 4; ++kk)
#pragma unroll
        for (int m = 0; m < 4; ++m) a2[m] = MF(vf[m][kk], wo[n][kk], a2[m]);
#pragma unroll
      for (int sg = 0; sg < 2; ++sg) {
        const f32x4 &a = a2[2 * sg], &b = a2[2 * sg + 1];
        u32x4 v = {pack2(a[0], a[1]), pack2(a[2], a[3]), pack2(b[0], b[1]), pack2(b[2], b[3])};
        st128(v, ro, lane * 16, (unsigned)(((tile * 2 + sg) * 32 + wave * 8 + n) * 32 + h) * 1024);
      }
    }
    if (tile == 0 || tile == 31) {
      WAITV(0);
      set_flag(rc, F_VW + (unsigned)((tile * 4 + wave) * 32 + h) * 4);
    }
  }
}

DI void out_worker(const Params& p, int cb, int ph, char* lds, const int wave) {
  const int lane = lane_id(), g = lane >> 4, r16 = lane & 15;
  LAS char* L = (LAS char*)lds;
  const rsrc_t rc = mkrsrc(p.ex + EX_FLAGS, F_END);
  unsigned budget = SPIN_BUDGET;
  {
    const int tile = cb * 4 + ph;
    const rsrc_t rx = mkrsrc(p.ws + WS_XN, 2 * MB);
    const int l32 = lane & 31;
    u32x4 raw[8][2];
#pragma unroll
    for (int it = 0; it < 8; ++it) {
      const int row = tile * 64 + wave * 16 + it * 2 + (lane >> 5);
#pragma unroll
      for (int j = 0; j < 2; ++j) raw[it][j] = *(const u32x4*)(p.x + (size_t)row * DM + (j * 32 + l32) * 8);
    }
#pragma unroll
    for (int it = 0; it < 8; ++it) {
      const int row = tile * 64 + wave * 16 + it * 2 + (lane >> 5);
      float v[16];
#pragma unroll
      for (int j = 0; j < 2; ++j)
#pragma unroll
        for (int e = 0; e < 4; ++e) { v[j * 8 + 2 * e] = __uint_as_float(raw[it][j][e] << 16); v[j * 8 + 2 * e + 1] = __uint_as_float(raw[it][j][e] & 0xffff0000u); }
      float sm = 0.f;
#pragma unroll
      for (int e = 0; e < 16; ++e) sm += v[e];
      const float mean = xsum16(rsum16(sm)) * (1.0f / DM);
      float q = 0.f;
#pragma unroll
      for (int e = 0; e < 16; ++e) { v[e] -= mean; q += v[e] * v[e]; }
      const float rstd = 1.0f / sqrtf(xsum16(rsum16(q)) * (1.0f / DM) + 1e-5f);
#pragma unroll
      for (int j = 0; j < 2; ++j) {
        u32x4 o;
#pragma unroll
        for (int e = 0; e < 4; ++e) o[e] = pack2(v[j * 8 + 2 * e] * rstd, v[j * 8 + 2 * e + 1] * rstd);
        st128(o, rx, (unsigned)(row * DM + (j * 32 + l32) * 8) * 2, 0);
      }
    }
    WAITV(0);
    barrier_raw();
    if (wave == 0) set_flag(rc, F_XN + tile * 4);
  }
  const rsrc_t rvw = mkrsrc(p.ws + WS_VW, 64 * MB);
  const rsrc_t rvo = mkrsrc(p.ws + WS_VO, 16 * MB);
  const rsrc_t rpt = mkrsrc(p.ws + WS_PT, 4 * MB);
  const rsrc_t rh = mkrsrc(p.ws + WS_H, 4 * MB);
  const unsigned lane16 = lane * 16;
  const unsigned vo_pm = (unsigned)(g * 2048 + 1536 + r16 * 16);
  const unsigned vo_vo = (unsigned)(g * 8192 + (cb * 64 + r16) * 16);
  const int orow = wave * 8 + (lane >> 4), oc = (lane & 15) * 4;
  for (int s = ph; s < NSEG; s += 4) {
    bf16x8 vw[8][4];
    wait_flags(rc, (lane & 7) * 4, F_VW + (unsigned)(((s >> 1) * 4 + (cb >> 1)) * 32 + wave * 8) * 4, budget);
#pragma unroll
    for (int t = 0; t < 4; ++t)
#pragma unroll
      for (int j = 0; j < 4; ++j) {
        const unsigned so = (unsigned)((s * 32 + cb * 4 + t) * 32 + wave * 8) * 1024;
        vw[j][t] = ldfrag(rvw, lane16 + j * 1024, so);
        vw[4 + j][t] = ldfrag(rvw, lane16 + j * 1024, so + 4096);
      }
    u32x2 xr[2];
#pragma unroll
    for (int ps = 0; ps < 2; ++ps) xr[ps] = *(const u32x2*)(p.x + (size_t)(s * 32 + orow + ps * 4) * DM + cb * 64 + oc);
    {
      const unsigned fo = lane < 16 ? F_PT + (unsigned)(s * NH + wave * 8) * 8 + lane * 4 : F_VO + (unsigned)((s * 4 + (cb >> 1)) * 32 + wave * 8 + (lane & 7)) * 4;
      wait_flags(rc, fo, 0, budget);
    }
    u32x2 p0[8];
    bf16x8 p1[8], pm[2][2], vo[2][4];
    const unsigned sp = (unsigned)(s * NH + wave * 8) * 2048;
#pragma unroll
    for (int j = 0; j < 8; ++j) { p1[j] = ldfrag(rpt, lane16 + 512, sp + j * 2048); p0[j] = ld64(rpt, lane * 8, sp + j * 2048); }
#pragma unroll
    for (int i = 0; i < 2; ++i)
#pragma unroll
      for (int qt = 0; qt < 2; ++qt) pm[i][qt] = ldfrag(rpt, vo_pm + qt * 256, sp + i * 8192);
#pragma unroll
    for (int i = 0; i < 2; ++i)
#pragma unroll
      for (int t = 0; t < 4; ++t) vo[i][t] = ldfrag(rvo, vo_vo + t * 256, (unsigned)(s * NH + wave * 8 + i * 4) * 8192);
    SCHED();
    f32x4 acc[4][2] = {};
#pragma unroll
    for (int j = 0; j < 8; ++j) {
      u32x4 z0 = {p0[j][0], p0[j][1], 0u, 0u};
      const bf16x8 b0 = __builtin_bit_cast(bf16x8, z0);
#pragma unroll
      for (int t = 0; t < 4; ++t) { acc[t][1] = MF(vw[j][t], p1[j], acc[t][1]); acc[t][0] = MF(vw[j][t], b0, acc[t][0]); }
    }
#pragma unroll
    for (int i = 0; i < 2; ++i)
#pragma unroll
      for (int t = 0; t < 4; ++t) { acc[t][0] = MF(vo[i][t], pm[i][0], acc[t][0]); acc[t][1] = MF(vo[i][t], pm[i][1], acc[t][1]); }
#pragma unroll
    for (int t = 0; t < 4; ++t)
#pragma unroll
      for (int qt = 0; qt < 2; ++qt) *(LAS f32x4*)(L + wave * 8704 + (qt * 16 + r16) * 272 + (t * 16 + g * 4) * 4) = acc[t][qt];
    barrier_raw();
#pragma unroll
    for (int ps = 0; ps < 2; ++ps) {
      f32x4 hv = {__uint_as_float(xr[ps][0] << 16), __uint_as_float(xr[ps][0] & 0xffff0000u), __uint_as_float(xr[ps][1] << 16), __uint_as_float(xr[ps][1] & 0xffff0000u)};
      LAS char* a = L + (orow + ps * 4) * 272 + oc * 4;
      f32x4 u0 = *(const LAS f32x4*)(a), u1 = *(const LAS f32x4*)(a + 8704), u2 = *(const LAS f32x4*)(a + 2 * 8704), u3 = *(const LAS f32x4*)(a + 3 * 8704);
      hv += (u0 + u1) + (u2 + u3);
      st128(__builtin_bit_cast(u32x4, hv), rh, (unsigned)(((orow + ps * 4) * DM + cb * 64 + oc) * 4), (unsigned)s * (32 * DM * 4));
    }
    WAITV(0);
    barrier_raw();
    if (wave == 0) set_flag(rc, F_H + (unsigned)s * 128 + cb * 4);
    if (cb == 0) RSTAMP(256 + s);
  }
}

DI float gelu_tanh(float x) {
  float t = x * (-2.302208198f - 0.1029432397f * x * x);
  return x * __builtin_amdgcn_rcpf(1.0f + __builtin_amdgcn_exp2f(t));
}

DI void f1_worker(const Params& p, int fb, int rh, char* lds, const int wave) {
  const int lane = lane_id(), g = lane >> 4, r16 = lane & 15;
  const int wr = wave >> 1, wc = wave & 1;
  LAS char* L = (LAS char*)lds;
  const unsigned lbase = __builtin_amdgcn_readfirstlane((unsigned)(size_t)L);
  const rsrc_t rc = mkrsrc(p.ex + EX_FLAGS, F_END);
  unsigned budget = SPIN_BUDGET;
#pragma unroll 4
  for (int i = 0; i < 32; ++i) {
    int blk = wave * 32 + i;
    int row = blk * 4 + (lane >> 4);
    int ch = (lane & 15) ^ (((row & 3) << 2) | ((row >> 2) & 3));
    int f = fb * 64 + (ch & 7) * 8;
    if (f >= DF) f = 0;
    glds(((ch >> 3) ? p.wu : p.wg) + (size_t)row * DF + f, lbase + blk * 1024);
  }
  WAITV(0);
  barrier_raw();
  bf16x8 wf[2][2][16];
  {
    const int q = r16 >> 2, pp = lane & 3;
#pragma unroll
    for (int up = 0; up < 2; ++up) {
      const unsigned o0 = off_b(8 * g + q, 8 * up + 4 * wc + pp), o1 = off_b(8 * g + 4 + q, 8 * up + 4 * wc + pp);
#pragma unroll
      for (int t = 0; t < 2; ++t)
#pragma unroll
        for (int kk = 0; kk < 16; ++kk) wf[up][t][kk] = tr_frag(L + o0 + kk * 8192 + 8 * t, L + o1 + kk * 8192 + 8 * t);
    }
  }
  barrier_raw();
  const rsrc_t rhh = mkrsrc(p.ws + WS_H, 4 * MB);
  const rsrc_t ra = mkrsrc(p.ws + WS_ACT, S_ * DFP * 2);
  const bool pad = fb * 64 + wc * 32 >= DF;
  for (int s = rh; s < NSEG; s += 2) {
    if (wave == 0) wait_flags(rc, (lane & 7) * 4, F_H + (unsigned)s * 128, budget);
    barrier_raw();
    {
      f32x4 hv[2][8];
#pragma unroll
      for (int it = 0; it < 2; ++it)
#pragma unroll
        for (int j = 0; j < 8; ++j)
          hv[it][j] = __builtin_bit_cast(f32x4, ld128(rhh, (unsigned)(((wave * 8 + it * 4 + g) * DM + (j * 16 + r16) * 4) * 4), (unsigned)s * (32 * DM * 4)));
      SCHED();
#pragma unroll
      for (int it = 0; it < 2; ++it) {
        const int row = wave * 8 + it * 4 + g;
        f32x4 s4 = (hv[it][0] + hv[it][1]) + (hv[it][2] + hv[it][3]) + ((hv[it][4] + hv[it][5]) + (hv[it][6] + hv[it][7]));
        const float mean = rsum16((s4[0] + s4[1]) + (s4[2] + s4[3])) * (1.0f / DM);
        f32x4 q4 = {0.f, 0.f, 0.f, 0.f};
#pragma unroll
        for (int j = 0; j < 8; ++j) { hv[it][j] -= mean; q4 += hv[it][j] * hv[it][j]; }
        const float rstd = __builtin_amdgcn_rsqf(rsum16((q4[0] + q4[1]) + (q4[2] + q4[3])) * (1.0f / DM) + 1e-5f);
#pragma unroll
        for (int j = 0; j < 8; ++j) {
          f32x4 y = hv[it][j] * rstd;
          u32x2 v = {pack2(y[0], y[1]), pack2(y[2], y[3])};
          int ch = j * 8 + (r16 >> 1);
          *(LAS u32x2*)(L + row * 1024 + ((ch ^ (row & 15)) * 16) + (r16 & 1) * 8) = v;
        }
      }
    }
    barrier_raw();
    f32x4 acc[2][2] = {};
    {
      bf16x8 xf[16];
#pragma unroll
      for (int kk = 0; kk < 16; ++kk) xf[kk] = *(const LAS bf16x8*)(L + (wr * 16 + r16) * 1024 + (((4 * kk + g) ^ r16) * 16));
      SCHED();
#pragma unroll
      for (int kk = 0; kk < 16; ++kk)
#pragma unroll
        for (int up = 0; up < 2; ++up)
#pragma unroll
          for (int t = 0; t < 2; ++t) acc[up][t] = MF(wf[up][t][kk], xf[kk], acc[up][t]);
    }
    {
      float o[8];
#pragma unroll
      for (int t = 0; t < 2; ++t)
#pragma unroll
        for (int r = 0; r < 4; ++r) o[t * 4 + r] = gelu_tanh(acc[0][t][r]) * acc[1][t][r];
      u32x4 v = {pack2(o[0], o[1]), pack2(o[2], o[3]), pack2(o[4], o[5]), pack2(o[6], o[7])};
      if (pad) { v[0] = 0u; v[1] = 0u; v[2] = 0u; v[3] = 0u; }
      st128(v, ra, lane * 16, (unsigned)((s * 2 + wr) * 44 + fb * 2 + wc) * 1024);
    }
    WAITV(0);
    barrier_raw();
    if (wave == 0) set_flag(rc, F_ACT + (unsigned)s * 128 + fb * 4);
    if (fb == 0) RSTAMP(320 + s);
  }
}

DI void f2_worker(const Params& p, int cb, int par2, char* lds, const int wave) {
  const int lane = lane_id(), g = lane >> 4, r16 = lane & 15;
  LAS char* L = (LAS char*)lds;
  const unsigned lbase = __builtin_amdgcn_readfirstlane((unsigned)(size_t)L);
  const rsrc_t rc = mkrsrc(p.ex + EX_FLAGS, F_END);
  unsigned budget = SPIN_BUDGET;
  bf16x8 wf[4][11];
#pragma unroll
  for (int hf = 0; hf < 2; ++hf) {
#pragma unroll 2
    for (int i = 0; i < 22; ++i) {
      int blk = wave * 22 + i;
      int row = blk * 16 + (lane >> 2);
      int rs = row < DF ? row : 0;
      glds(p.wd + (size_t)rs * DM + cb * 64 + hf * 32 + (lane & 3) * 8, lbase + blk * 1024);
    }
    WAITV(0);
    const int q = r16 >> 2, pp = lane & 3;
#pragma unroll
    for (int t = 0; t < 2; ++t)
#pragma unroll
      for (int kk = 0; kk < 11; ++kk) {
        int row = 32 * (wave * 11 + kk) + 8 * g + q;
        wf[hf * 2 + t][kk] = tr_frag(L + row * 64 + t * 32 + pp * 8, L + (row + 4) * 64 + t * 32 + pp * 8);
      }
    LGKM0();
  }
  barrier_raw();
  const rsrc_t rh = mkrsrc(p.ws + WS_H, 4 * MB);
  const rsrc_t ra = mkrsrc(p.ws + WS_ACT, S_ * DFP * 2);
  const int orow = wave * 8 + (lane >> 3), oc = (lane & 7) * 8;
  for (int s = par2; s < NSEG; s += 2) {
    if (wave == 0) wait_flags(rc, (lane < 22 ? lane : 0) * 4, F_ACT + (unsigned)s * 128, budget);
    barrier_raw();
    f32x4 acc[2][4] = {};
    bf16x8 xf[2][11];
#pragma unroll
    for (int kk = 0; kk < 11; ++kk)
#pragma unroll
      for (int m = 0; m < 2; ++m) xf[m][kk] = ldfrag(ra, lane * 16, (unsigned)((s * 2 + m) * 44 + wave * 11 + kk) * 1024);
    const unsigned hoff = (unsigned)((orow * DM + cb * 64 + oc) * 4);
    f32x4 h0 = __builtin_bit_cast(f32x4, ld128(rh, hoff, (unsigned)s * (32 * DM * 4)));
    f32x4 h1 = __builtin_bit_cast(f32x4, ld128(rh, hoff + 16, (unsigned)s * (32 * DM * 4)));
    SCHED();
#pragma unroll
    for (int kk = 0; kk < 11; ++kk)
#pragma unroll
      for (int m = 0; m < 2; ++m)
#pragma unroll
        for (int t = 0; t < 4; ++t) acc[m][t] = MF(wf[t][kk], xf[m][kk], acc[m][t]);
#pragma unroll
    for (int m = 0; m < 2; ++m)
#pragma unroll
      for (int t = 0; t < 4; ++t) *(LAS f32x4*)(L + wave * 8704 + (m * 16 + r16) * 272 + (t * 16 + g * 4) * 4) = acc[m][t];
    barrier_raw();
#pragma unroll
    for (int w = 0; w < 4; ++w) {
      h0 += *(const LAS f32x4*)(L + w * 8704 + orow * 272 + oc * 4);
      h1 += *(const LAS f32x4*)(L + w * 8704 + orow * 272 + oc * 4 + 16);
    }
    u32x4 v = {pack2(h0[0], h0[1]), pack2(h0[2], h0[3]), pack2(h1[0], h1[1]), pack2(h1[2], h1[3])};
    *(u32x4*)(p.out + ((size_t)s * 32 + orow) * DM + cb * 64 + oc) = v;
    if (cb == 0) RSTAMP(384 + s);
  }
}

__global__ __launch_bounds__(256) void k_fill(Params p) {
  const u32x4 sent = {0xffffffffu, 0xffffffffu, 0xffffffffu, 0xffffffffu};
  const unsigned i = blockIdx.x * 256 + threadIdx.x;
  if (i < EX_TOTAL / 16) ((u32x4*)p.ex)[i] = sent;
}

__global__ __launch_bounds__(256, 1) void k_mega(Params p) {
  extern __shared__ __attribute__((aligned(16))) char lds[];
  const int b = blockIdx.x;
  const int x = b & 7, c = b >> 3;
  const int wave = __builtin_amdgcn_readfirstlane(threadIdx.x >> 6);
  if (b == 0) RSTAMP(200);
  if (x < 2) {
    if (c < 16) a_head(p, c * 2 + x, lds, wave);
    else v_head(p, (c - 16) * 2 + x, lds, wave);
  } else if (x < 4) {
    if (c < 16) {
      if (b == 2) calibrate(p, lds, wave);
      reducer(p, c * 2 + (x - 2), lds, wave);
    } else qk_worker(p, (c - 16) * 2 + (x - 2), lds, wave);
  } else {
    const int y = x - 4;
    if (c < 8) vw_worker(p, c * 4 + y, lds, wave);
    else if (c < 16) out_worker(p, c - 8, y, lds, wave);
    else if (c < 27) f1_worker(p, (c - 16) + 11 * (y & 1), y >> 1, lds, wave);
    else if (c < 31) f2_worker(p, (c - 27) + 4 * (y & 1), y >> 1, lds, wave);
  }
}

extern "C" void kernel_launch(void* const* d_in, const int* in_sizes, int n_in, void* d_out, int out_size, void* d_ws,
                              size_t ws_size, hipStream_t stream) {
  Params p;
  p.x = (const u16*)d_in[0]; p.mem0 = (const u16*)d_in[1]; p.wq = (const u16*)d_in[2]; p.wk = (const u16*)d_in[3];
  p.wv = (const u16*)d_in[4]; p.wo = (const u16*)d_in[5];
  p.wg = (const u16*)d_in[8]; p.wu = (const u16*)d_in[9]; p.wd = (const u16*)d_in[10];
  p.out = (u16*)d_out; p.ws = (char*)d_ws;
  uintptr_t exa = ((uintptr_t)d_ws + WS_EX_MIN + ((2u << 20) - 1)) & ~(uintptr_t)((2u << 20) - 1);
  p.ex = (char*)exa;
  (void)hipFuncSetAttribute((const void*)k_mega, hipFuncAttributeMaxDynamicSharedMemorySize, L_TOTAL);
  k_fill<<<(EX_TOTAL / 16 + 255) / 256, 256, 0, stream>>>(p);
  k_mega<<<256, 256, L_TOTAL, stream>>>(p);
}
```

```cpp
#include <hip/hip_runtime.h>
#include <hip/hip_bf16.h>
#include <stdint.h>

constexpr int S_ = 2048, DM = 512, NH = 32, HD = 4096, NSEG = 64, DF = 1376, DFP = 1408;

typedef __attribute__((ext_vector_type(8))) __bf16 bf16x8;
typedef __attribute__((ext_vector_type(16))) __bf16 bf16x16;
typedef __attribute__((ext_vector_type(2))) __bf16 bf16x2;
typedef __attribute__((ext_vector_type(4))) short s16x4;
typedef __attribute__((ext_vector_type(8))) short s16x8;
typedef __attribute__((ext_vector_type(16))) short s16x16;
typedef __attribute__((ext_vector_type(4))) float f32x4;
typedef __attribute__((ext_vector_type(4))) unsigned u32x4;
typedef __attribute__((ext_vector_type(2))) unsigned u32x2;
typedef unsigned short u16;
typedef __amdgpu_buffer_rsrc_t rsrc_t;
#define LAS __attribute__((address_space(3)))
#define DI __device__ __forceinline__
template <int N> struct IC { static constexpr int v = N; };

constexpr size_t MB = 1u << 20;
constexpr size_t WS_XN = 0;
constexpr size_t WS_Q = 2 * MB;
constexpr size_t WS_K = WS_Q + 16 * MB;
constexpr size_t WS_VW = WS_K + 16 * MB;
constexpr size_t WS_VO = WS_VW + 64 * MB;
constexpr size_t WS_PT = WS_VO + 16 * MB;
constexpr size_t WS_H = WS_PT + 4 * MB;
constexpr size_t WS_ACT = WS_H + 4 * MB;
constexpr size_t WS_EX_MIN = WS_ACT + 6 * MB;
constexpr unsigned EX_VO = 0;
constexpr unsigned EX_P = 524288;
constexpr unsigned EX_MEM = EX_P + 131072;
constexpr unsigned EX_CAL = EX_MEM + 65536;
constexpr unsigned EX_LEND = EX_CAL + 16384;
constexpr unsigned EX_FLAGS = 2 * EX_LEND;
constexpr unsigned F_PAR = 0;
constexpr unsigned F_GO = 128;
constexpr unsigned F_QK = 256;
constexpr unsigned F_VW = F_QK + 32768;
constexpr unsigned F_PT = F_VW + 32768;
constexpr unsigned F_VO = F_PT + 16384;
constexpr unsigned F_H = F_VO + 32768;
constexpr unsigned F_ACT = F_H + 8192;
constexpr unsigned F_REC = F_ACT + 8192;
constexpr unsigned F_ARM = F_REC + 66 * 128;
constexpr unsigned F_XN = F_ARM + 60 * 128;
constexpr unsigned F_IEND = (F_XN + 32 * 128 + 1023) & ~1023u;
constexpr unsigned H_R = F_IEND;
constexpr unsigned H_A = H_R + 32768;
constexpr unsigned F_END = H_A + 65536;
static_assert(EX_FLAGS + F_END <= 2 * MB, "exchange block");
#define FPT(s, h, w) (F_PT + (unsigned)((h) * 64 + (s)) * 8 + (w) * 4)
#define FVO(s, w, h) (F_VO + (unsigned)(((h) * 64 + (s)) * 4 + (w)) * 4)
#define FVW(s, w, h) (F_VW + (unsigned)(((h) * 64 + (s)) * 4 + (w)) * 4)
constexpr int L_TOTAL = 160 * 1024;
constexpr unsigned OOB = 0x7fffffffu;

DI unsigned exphys(unsigned L, unsigned par) { return ((L >> 13) << 14) | (L & 8191u) | (par << 13); }
DI unsigned pack2(float a, float b) {
  bf16x2 v; v[0] = (__bf16)a; v[1] = (__bf16)b;
  return __builtin_bit_cast(unsigned, v);
}
DI unsigned off_b(unsigned row, unsigned ch) { return 256u * row + 16u * (ch ^ (((row & 3) << 2) | ((row >> 2) & 3))); }
DI s16x4 tr_read_l(LAS char* a) { return __builtin_amdgcn_ds_read_tr16_b64_v4i16((LAS s16x4*)a); }
DI bf16x8 tr_frag(LAS char* a0, LAS char* a1) {
  s16x4 lo = tr_read_l(a0), hi = tr_read_l(a1);
  return __builtin_bit_cast(bf16x8, __builtin_shufflevector(lo, hi, 0, 1, 2, 3, 4, 5, 6, 7));
}
#define WPOL ""
DI void glds(const void* g, unsigned lds_dst) {
  unsigned keep;
  asm volatile("s_mov_b32 %0, m0\n\ts_mov_b32 m0, %2\n\ts_nop 0\n\tglobal_load_lds_dwordx4 %1, off" WPOL "\n\ts_mov_b32 m0, %0" : "=&s"(keep) : "v"(g), "s"(lds_dst) : "memory");
}
DI void glds_c(const void* g, unsigned lds_dst) {
  unsigned keep;
  asm volatile("s_mov_b32 %0, m0\n\ts_mov_b32 m0, %2\n\ts_nop 0\n\tglobal_load_lds_dwordx4 %1, off sc1\n\ts_mov_b32 m0, %0" : "=&s"(keep) : "v"(g), "s"(lds_dst) : "memory");
}
DI int lane_id() { return __builtin_amdgcn_mbcnt_hi(~0u, __builtin_amdgcn_mbcnt_lo(~0u, 0u)); }
DI void barrier_raw() {
  asm volatile("s_waitcnt lgkmcnt(0)" ::: "memory");
  __builtin_amdgcn_s_barrier();
  asm volatile("" ::: "memory");
}
#define WAITV(n) asm volatile("s_waitcnt vmcnt(" #n ")" ::: "memory")
#define LGKM0() asm volatile("s_waitcnt lgkmcnt(0)" ::: "memory")
#define PIN_A(x) asm volatile("" : "+a"(x))
#define PIN_V(x) asm volatile("" : "+v"(x))
#define SCHED() __builtin_amdgcn_sched_barrier(0)
#define SMF(a, b, c) __builtin_amdgcn_smfmac_f32_16x16x64_bf16((a), (b), (c), sidx, 0, 0)
#define MF(a, b, c) __builtin_amdgcn_mfma_f32_16x16x32_bf16((a), (b), (c), 0, 0, 0)
#define MFV0(acc, a, b) asm volatile("v_mfma_f32_16x16x32_bf16 %0, %1, %2, 0" : "=&v"(acc) : "v"(a), "v"(b))
#define MFV(acc, a, b) asm volatile("v_mfma_f32_16x16x32_bf16 %0, %1, %2, %0" : "+v"(acc) : "v"(a), "v"(b))
DI float vmax(float a, float b) { float r; asm("v_max_f32 %0, %1, %2" : "=v"(r) : "v"(a), "v"(b)); return r; }
DI float vmax3(float a, float b, float c) { float r; asm("v_max3_f32 %0, %1, %2, %3" : "=v"(r) : "v"(a), "v"(b), "v"(c)); return r; }
DI rsrc_t mkrsrc(const void* p, unsigned bytes) { return __builtin_amdgcn_make_buffer_rsrc((void*)p, 0, bytes, 0x00020000); }
DI u32x4 ld128(rsrc_t r, unsigned v, unsigned s) { return __builtin_amdgcn_raw_buffer_load_b128(r, v, s, 16); }
DI u32x2 ld64(rsrc_t r, unsigned v, unsigned s) { return __builtin_amdgcn_raw_buffer_load_b64(r, v, s, 16); }
DI bf16x8 ldfrag(rsrc_t r, unsigned v, unsigned s) { return __builtin_bit_cast(bf16x8, ld128(r, v, s)); }
DI void st128(u32x4 x, rsrc_t r, unsigned v, unsigned s) { __builtin_amdgcn_raw_buffer_store_b128(x, r, v, s, 16); }
DI void st64(u32x2 x, rsrc_t r, unsigned v, unsigned s) { __builtin_amdgcn_raw_buffer_store_b64(x, r, v, s, 16); }
DI void st32(unsigned x, rsrc_t r, unsigned v, unsigned s) { __builtin_amdgcn_raw_buffer_store_b32(x, r, v, s, 16); }

#define WFROM 20
#define NARROW 18
#define XA 1
#define XB 2
#define VFN 1
#define VMID 4
#define KH 8
#define SWAPO 0
static_assert(VMID >= 1, "first flag");
#define FARW 4
#define PUBF 0
#define E0 244
#define EMAX 218
#define OAUX 16
#define F2S 55
#define QA0 1467.f
#define QB0 1505.f
#define SCA 1.08f
#define SCB 1.25f
#define RLATE 1
#define AEARLY 0
#define WT 650u
#define WT2 750u
#define PKE 12
#define QPD 6
#define QMID 13
#define TO 22u
#define TH 158u
#define VK 6
#define BLK 1
#define RMK 3
#define SA0 1
#define VDEL 120u
struct Params {
  const u16* x; const u16* mem0; const u16* wq; const u16* wk; const u16* wv; const u16* wo;
  const u16* wg; const u16* wu; const u16* wd;
  u16* out; char* ws; char* ex;
};

DI Params args_again() {
  typedef const __attribute__((address_space(4))) Params* cp;
  cp q = (cp)__builtin_amdgcn_kernarg_segment_ptr();
  asm volatile("" : "+s"(q) :: "memory");
  Params r;
  r.x = q->x; r.mem0 = q->mem0; r.wq = q->wq; r.wk = q->wk; r.wv = q->wv; r.wo = q->wo; r.wg = q->wg; r.wu = q->wu; r.wd = q->wd;
  r.out = q->out; r.ws = q->ws; r.ex = q->ex;
  return r;
}
DI float dpp_ror8(float x) { return __int_as_float(__builtin_amdgcn_update_dpp(0, __float_as_int(x), 0x128, 0xf, 0xf, false)); }
DI unsigned dpp_ror8u(unsigned x) { return (unsigned)__builtin_amdgcn_update_dpp(0, (int)x, 0x128, 0xf, 0xf, false); }
DI float rsum16(float x) {
  x += dpp_ror8(x);
  x += __int_as_float(__builtin_amdgcn_update_dpp(0, __float_as_int(x), 0x124, 0xf, 0xf, false));
  x += __int_as_float(__builtin_amdgcn_update_dpp(0, __float_as_int(x), 0x122, 0xf, 0xf, false));
  x += __int_as_float(__builtin_amdgcn_update_dpp(0, __float_as_int(x), 0x121, 0xf, 0xf, false));
  return x;
}
DI unsigned umax4(u32x4 v) { return max(max(v[0], v[1]), max(v[2], v[3])); }
DI unsigned nosent(unsigned v) { return min(v, 0xfffffffeu); }
DI unsigned umax4f(bf16x8 v) { return umax4(__builtin_bit_cast(u32x4, v)); }
DI u32x4 nosent4(u32x4 v) { u32x4 r = {nosent(v[0]), nosent(v[1]), nosent(v[2]), nosent(v[3])}; return r; }
DI void swap_add(const f32x4& a, const f32x4& b_in, f32x4& out) {
#pragma unroll
  for (int r = 0; r < 4; ++r) {
    auto sw = __builtin_amdgcn_permlane32_swap(__float_as_uint(a[r]), __float_as_uint(b_in[r]), false, false);
    out[r] = __uint_as_float(sw[0]) + __uint_as_float(sw[1]);
  }
}
DI float xmax16(float x) {
  auto a = __builtin_amdgcn_permlane16_swap(__float_as_uint(x), __float_as_uint(x), false, false);
  return vmax(__uint_as_float(a[0]), __uint_as_float(a[1]));
}
DI float xmax32(float x) {
  auto b = __builtin_amdgcn_permlane32_swap(__float_as_uint(x), __float_as_uint(x), false, false);
  return vmax(__uint_as_float(b[0]), __uint_as_float(b[1]));
}
DI float xsum16(float x) {
  auto a = __builtin_amdgcn_permlane16_swap(__float_as_uint(x), __float_as_uint(x), false, false);
  return __uint_as_float(a[0]) + __uint_as_float(a[1]);
}
DI float xsum32(float x) {
  auto b = __builtin_amdgcn_permlane32_swap(__float_as_uint(x), __float_as_uint(x), false, false);
  return __uint_as_float(b[0]) + __uint_as_float(b[1]);
}

constexpr unsigned SPIN_BUDGET = 1u << 21;
DI void wait_flags(rsrc_t rc, unsigned voff, unsigned soff, unsigned& budget) {
  for (;;) {
    asm volatile("" ::: "memory");
    unsigned v = __builtin_amdgcn_raw_buffer_load_b32(rc, voff, soff, 16);
    if (__all(v != 0xffffffffu) || budget == 0) break;
    --budget;
    __builtin_amdgcn_s_sleep(3);
  }
  asm volatile("" ::: "memory");
}
#define PSL 3
#define LAZY 20
DI unsigned wait_flags_lazy(rsrc_t rc, unsigned voff, unsigned soff, unsigned& budget, bool lazy) {
  unsigned v;
  for (;;) {
    asm volatile("" ::: "memory");
    v = __builtin_amdgcn_raw_buffer_load_b32(rc, voff, soff, 16);
    if (__all(v != 0xffffffffu) || budget == 0) break;
    --budget;
    if (lazy) __builtin_amdgcn_s_sleep(LAZY); else __builtin_amdgcn_s_sleep(PSL);
  }
  asm volatile("" ::: "memory");
  return v;
}
DI void set_flag(rsrc_t rc, unsigned soff) {
  st32(1u, rc, lane_id() == 0 ? 0u : OOB, soff);
}
DI void hold(unsigned t0, unsigned d) {
  for (int i = 0; i < 256; ++i) { if ((unsigned)__builtin_amdgcn_s_memtime() - t0 >= d) break; __builtin_amdgcn_s_sleep(1); }
}
DI unsigned now32() { return (unsigned)__builtin_amdgcn_s_memrealtime(); }
DI void announce(rsrc_t rc, bool on, unsigned soff, unsigned d) { st32(nosent(now32() + d), rc, on && lane_id() == 0 ? 0u : OOB, soff); }
DI void wait_due(unsigned v) { for (int i = 0; i < 160; ++i) if (!__any(v - now32() - 1u < 400u)) break; }
struct Rate { unsigned c, r; };
DI unsigned rate(Rate& b) {
  const unsigned c = (unsigned)__builtin_amdgcn_s_memtime(), r = now32();
  const unsigned q = (unsigned)((float)(c - b.c) * 64.f * __builtin_amdgcn_rcpf((float)(r - b.r)));
  b.c = c; b.r = r;
  return min(__builtin_amdgcn_readfirstlane(q), 0x7fffu);
}
DI unsigned stretch(float t, float q0, unsigned q) {
  return __builtin_amdgcn_readfirstlane((unsigned)min(max(t * q0 * __builtin_amdgcn_rcpf((float)q) - t + 0.5f, 0.f), 16.f));
}
#define REC_ASK(r, off) asm volatile("s_buffer_load_dword %0, %1, %2" : "=&s"(r) : "s"(mkdesc(p.ex + EX_FLAGS, F_END)), "s"(F_REC + (off)) : "memory")
#define REC_GET(r) asm volatile("s_waitcnt lgkmcnt(0)" : "+s"(r) :: "memory")
DI void sched_wait(unsigned rec, unsigned t0, unsigned d) {
  rec = __builtin_amdgcn_readfirstlane(rec);
  if (rec == 0xffffffffu) { hold(t0, d); return; }
  unsigned prev = now32();
  for (int i = 0; i < 96; ++i) {
    const unsigned v = now32();
    int r = (int)(rec - v);
    if (r <= 0 || r > 250) break;
    if (r < 4 && v != prev) {
      asm volatile("1:\n\ts_sub_u32 %0, %0, 1\n\ts_cbranch_scc0 1b" : "+s"(r) :: "scc");
      break;
    }
    prev = v;
  }
}
#define PEEKN 3
#define REB 6
#define EMIN 212
#define ESL 12
#define GATE0 70
#define GATE1 80
#define GEND 16
#define SDX 0
#define NH2 6
#define OHJ 2
#define OD 100u
#define FD 138u
#define NARM 9
#define NLAZY 9
#define QUIET_A 11
#define QUIET_T 0
#define QUIET_R 10
#define QUIET_R0 4
#define GOODN 1
#define UPS 1
#define TA0 94
#define TB0 75
#define DA0 1800
#define DA1 2900
#define DV 1500
#define DR 2600
#define LNDEL 480u
#define CALN 2
#define HSD 250u
#define HSP 6
#define HSS 12
struct Hs { unsigned lo, hi, blk; };
DI bool hs_eq(u32x2 v, const Hs& h) { return __all(v[0] == h.lo && v[1] == h.hi); }
DI bool hs_try(rsrc_t rc, const Hs& h) {
  asm volatile("" ::: "memory");
  const u32x2 v = ld64(rc, (unsigned)(lane_id() & 31) * 2048, H_A + h.blk * 8);
  return hs_eq(v, h);
}
DI void hs_post(rsrc_t rc, const Hs& h, unsigned flip) {
  asm volatile("" ::: "memory");
  const u32x2 d = {h.lo ^ flip, h.hi ^ flip};
  st64(d, rc, lane_id() == 0 ? 0u : OOB, H_R + h.blk * 128);
}
DI void hs_spin(rsrc_t rc, const Hs& h, unsigned& budget) {
  while (!hs_try(rc, h) && budget != 0) { --budget; __builtin_amdgcn_s_sleep(HSP); }
  asm volatile("" ::: "memory");
}
DI void hs_wait(rsrc_t rc, const Hs& h, const int wave, unsigned& budget) {
  if (wave != 0) return;
  for (int i = 0; i < 64 && now32() - h.lo < HSD; ++i) __builtin_amdgcn_s_sleep(8);
  hs_spin(rc, h, budget);
  hs_post(rc, h, ~0u);
}
template <class F>
DI void hs_serve(rsrc_t rc, const Hs& h, int m, const int wave, u32x2 l, F&& meanwhile) {
  bool todo = true;
  const unsigned blk = wave * 64 + lane_id();
  st64(l, rc, blk * 8, H_A + m * 2048);
  bool done = blk >= 160 && (blk & 31) == 31, mine = wave != 0;
  for (unsigned it = 0; it < SPIN_BUDGET && !(mine && __all(done)); ++it) {
    __builtin_amdgcn_s_sleep(HSS);
    if (!mine && hs_try(rc, h)) { mine = true; hs_post(rc, h, ~0u); }
    asm volatile("" ::: "memory");
    const u32x2 r = ld64(rc, done ? OOB : blk * 128, H_R);
    bool ch = false;
    if (!done) { if (r[0] == ~l[0] && r[1] == ~l[1]) done = true; else if (r[0] != l[0] || r[1] != l[1]) { l = r; ch = true; } }
    st64(l, rc, ch ? blk * 8 : OOB, H_A + m * 2048);
    if (todo && now32() - h.lo >= WT) { todo = false; meanwhile(); }
  }
  if (todo) meanwhile();
  barrier_raw();
}
DI void ln_pair(u32x4 (&raw)[2]) {
  float v[16];
#pragma unroll
  for (int j = 0; j < 2; ++j)
#pragma unroll
    for (int e = 0; e < 4; ++e) { v[j * 8 + 2 * e] = __uint_as_float(raw[j][e] << 16); v[j * 8 + 2 * e + 1] = __uint_as_float(raw[j][e] & 0xffff0000u); }
  float sm = 0.f;
#pragma unroll
  for (int e = 0; e < 16; ++e) sm += v[e];
  const float mean = xsum16(rsum16(sm)) * (1.0f / DM);
  float q = 0.f;
#pragma unroll
  for (int e = 0; e < 16; ++e) { v[e] -= mean; q += v[e] * v[e]; }
  const float rstd = 1.0f / sqrtf(xsum16(rsum16(q)) * (1.0f / DM) + 1e-5f);
#pragma unroll
  for (int j = 0; j < 2; ++j)
#pragma unroll
    for (int e = 0; e < 4; ++e) raw[j][e] = pack2(v[j * 8 + 2 * e] * rstd, v[j * 8 + 2 * e + 1] * rstd);
}
template <int N>
DI void ln_rows(const Params& p, int row0) {
  const int lane = lane_id(), l32 = lane & 31;
  const rsrc_t rx = mkrsrc(p.ws + WS_XN, 2 * MB);
  u32x4 raw[N][2];
#pragma unroll
  for (int it = 0; it < N; ++it) {
    const int row = row0 + it * 2 + (lane >> 5);
#pragma unroll
    for (int j = 0; j < 2; ++j) raw[it][j] = *(const u32x4*)(p.x + (size_t)row * DM + (j * 32 + l32) * 8);
  }
#pragma unroll
  for (int it = 0; it < N; ++it) {
    const int row = row0 + it * 2 + (lane >> 5);
    ln_pair(raw[it]);
#pragma unroll
    for (int j = 0; j < 2; ++j) st128(raw[it][j], rx, (unsigned)(row * DM + (j * 32 + l32) * 8) * 2, 0);
  }
}
DI void ln_rest(const Params& p, const Hs& hs, int j, const int wave, unsigned& budget) {
  const rsrc_t rc = mkrsrc(p.ex + EX_FLAGS, F_END);
  for (int i = 0; i < 256 && now32() - hs.lo < LNDEL; ++i) __builtin_amdgcn_s_sleep(4);
  ln_rows<7>(p, 256 + j * 56 + wave * 14);
  WAITV(0);
  hs_wait(rc, hs, wave, budget);
  barrier_raw();
  if (wave == 0) set_flag(rc, F_XN + j * 128);
}
DI void arm_share(const Params& p, int j, const Hs& hs, const int wave, unsigned& budget) {
  const rsrc_t rex = mkrsrc(p.ex, EX_FLAGS);
  const rsrc_t rc = mkrsrc(p.ex + EX_FLAGS, F_END);
  const u32x4 sent = {0xffffffffu, 0xffffffffu, 0xffffffffu, 0xffffffffu};
  for (unsigned i = j * 4 + wave; i < 2 * EX_CAL / 1024; i += 112) st128(sent, rex, lane_id() * 16, i * 1024);
  WAITV(0);
  hs_wait(rc, hs, wave, budget);
  barrier_raw();
  if (wave == 0) set_flag(rc, F_ARM + j * 128);
}
DI unsigned wait_par(rsrc_t rc, unsigned& budget) {
  unsigned v;
  for (;;) {
    asm volatile("" ::: "memory");
    v = __builtin_amdgcn_readfirstlane(__builtin_amdgcn_raw_buffer_load_b32(rc, 0, F_PAR, 16));
    if (v != 0xffffffffu || budget == 0) break;
    --budget;
    __builtin_amdgcn_s_sleep(4);
  }
  asm volatile("" ::: "memory");
  return v & 3u;
}
DI void calibrate(const Params& p, const Hs& hs, char* lds, const int wave, unsigned& budget) {
  const int lane = lane_id();
  LAS unsigned* tm = (LAS unsigned*)lds;
  const rsrc_t r = mkrsrc(p.ex, EX_FLAGS);
  const rsrc_t rc = mkrsrc(p.ex + EX_FLAGS, F_END);
  const u32x4 one = {1u, 1u, 1u, 1u};
#pragma unroll
  for (int c = 0; c < 4; ++c) st128(one, r, exphys(EX_CAL + (c >> 1) * 8192 + wave * 2048 + lane * 16, c & 1), 0);
  asm volatile("s_waitcnt vmcnt(0)" ::: "memory");
  unsigned best[4] = {0xffffu, 0xffffu, 0xffffu, 0xffffu};
  for (int rep = 0; rep < 8; ++rep) {
#pragma unroll
    for (int c = 0; c < 4; ++c) {
      const unsigned off = exphys(EX_CAL + (c >> 1) * 8192 + wave * 2048 + rep * 128, c & 1);
      const unsigned t0 = (unsigned)__builtin_amdgcn_s_memtime();
      asm volatile("s_waitcnt lgkmcnt(0)" ::: "memory");
      unsigned x = __builtin_amdgcn_raw_buffer_load_b32(r, off, 0, 16);
      asm volatile("s_waitcnt vmcnt(0)" :: "v"(x) : "memory");
      best[c] = min(best[c], (unsigned)__builtin_amdgcn_s_memtime() - t0);
    }
    const int df = (int)max(best[0], best[2]) - (int)min(best[1], best[3]), dr = (int)max(best[1], best[3]) - (int)min(best[0], best[2]);
    if (rep >= CALN && (df < -100 || dr < -100)) break;
  }
  if (lane == 0) { tm[wave * 4] = best[0]; tm[wave * 4 + 1] = best[1]; tm[wave * 4 + 2] = best[2]; tm[wave * 4 + 3] = best[3]; }
  hs_wait(rc, hs, wave, budget);
  barrier_raw();
  if (wave == 0) {
    u32x4 m0[8];
#pragma unroll
    for (int i = 0; i < 8; ++i) m0[i] = *(const u32x4*)(p.mem0 + (lane & 7) * DM + (i * 8 + (lane >> 3)) * 8);
    wait_flags(rc, (lane < 28 ? lane : 0) * 128, F_ARM, budget);
    unsigned m[4];
#pragma unroll
    for (int c = 0; c < 4; ++c) m[c] = tm[c] + tm[4 + c] + tm[8 + c] + tm[12 + c];
    const unsigned pr = m[1] + m[3] < m[0] + m[2] ? 1u : 0u;
    const unsigned sd = (pr ? m[3] < m[1] : m[2] < m[0]) ? 1u : 0u;
    st32(pr | ((sd ^ SDX) << 1), rc, lane == 0 ? 0u : OOB, F_PAR);
#pragma unroll
    for (int i = 0; i < 16; ++i) st128(nosent4(m0[i & 7]), r, lane * 16, exphys(EX_MEM + i * 1024, __builtin_amdgcn_readfirstlane(pr)));
  }
  barrier_raw();
}

constexpr int L_XB = 0;
constexpr int L_QK = 16384;
constexpr int L_FL = 20480;
constexpr int L_ST = 32768;
#define FENCE() asm volatile("" ::: "memory")
#define FL_SET(off) do { FENCE(); *(volatile LAS unsigned*)(flw + (off)) = T; FENCE(); } while (0)
#define FL_GET(off) (*(volatile LAS unsigned*)(flr + (off)))
typedef __attribute__((ext_vector_type(8))) unsigned u32x8;
typedef __attribute__((ext_vector_type(16))) unsigned u32x16;
typedef __attribute__((ext_vector_type(32))) unsigned u32x32;
DI u32x32 cat4(bf16x16 a, bf16x16 b, bf16x16 c, bf16x16 d) {
  u32x16 h0 = __builtin_shufflevector(__builtin_bit_cast(u32x8, a), __builtin_bit_cast(u32x8, b), 0, 1, 2, 3, 4, 5, 6, 7, 8, 9, 10, 11, 12, 13, 14, 15);
  u32x16 h1 = __builtin_shufflevector(__builtin_bit_cast(u32x8, c), __builtin_bit_cast(u32x8, d), 0, 1, 2, 3, 4, 5, 6, 7, 8, 9, 10, 11, 12, 13, 14, 15);
  return __builtin_shufflevector(h0, h1, 0, 1, 2, 3, 4, 5, 6, 7, 8, 9, 10, 11, 12, 13, 14, 15, 16, 17, 18, 19, 20, 21, 22, 23, 24, 25, 26, 27, 28, 29, 30, 31);
}
DI u32x16 cat4f(const bf16x8* f) {
  u32x8 h0 = __builtin_shufflevector(__builtin_bit_cast(u32x4, f[0]), __builtin_bit_cast(u32x4, f[1]), 0, 1, 2, 3, 4, 5, 6, 7);
  u32x8 h1 = __builtin_shufflevector(__builtin_bit_cast(u32x4, f[2]), __builtin_bit_cast(u32x4, f[3]), 0, 1, 2, 3, 4, 5, 6, 7);
  return __builtin_shufflevector(h0, h1, 0, 1, 2, 3, 4, 5, 6, 7, 8, 9, 10, 11, 12, 13, 14, 15);
}
DI u32x4 mkdesc(const void* ptr, unsigned bytes) {
  const unsigned long long a = (unsigned long long)ptr;
  u32x4 d = {(unsigned)a, (unsigned)(a >> 32) & 0xffffu, bytes, 0x00020000u};
  return d;
}
DI unsigned laddr(LAS char* q) { return (unsigned)(size_t)q; }
#define A_STORE "buffer_store_dwordx2 v[220:221], v74, %[d], %[sp] offen sc1\n\tbuffer_store_dword v238, v75, %[d], %[sp] offen sc1\n\t"
#define V_STORE0 "buffer_store_dwordx4 v[220:223], v75, %[ds], %[sr] offen sc1\n\t"
#define VSLEEP "s_sleep 12\n\t"
#define V_STORE1 "buffer_store_dwordx4 v[228:231], v75, %[ds], %[sr] offen offset:1024 sc1\n\tbuffer_load_dword v250, off, %[d3], %[s3] sc1\n\t" VSLEEP \
                 "buffer_store_dwordx4 v[220:223], v75, %[d2], %[s2] offen sc1\n\tbuffer_store_dwordx4 v[228:231], v75, %[d2], %[s2] offen offset:1024 sc1\n\t"
#define RSPL "s_sleep 3\n\t"
#define R_LEAD "buffer_load_dwordx4 v[132:135], v66, %[d], %[sv0] offen sc1\n\tbuffer_load_dwordx4 v[136:139], v66, %[d], %[sv1] offen sc1\n\t" RSPL "s_branch 13f\n\t"
#define ASM_A0 \
  "s_branch 9f\n\t" \
  "30:\n\t" \
  "s_waitcnt lgkmcnt(0)\n\t" \
  "ds_read_b32 v225, v72 offset:1024\n\t" \
  "ds_read_b64_tr_b16 v[176:177], v70 offset:0\n\t" \
  "ds_read_b64_tr_b16 v[178:179], v70 offset:128\n\t" \
  "ds_read_b64_tr_b16 v[180:181], v70 offset:1024\n\t" \
  "ds_read_b64_tr_b16 v[182:183], v70 offset:1152\n\t" \
  "ds_read_b64_tr_b16 v[184:185], v70 offset:2048\n\t" \
  "ds_read_b64_tr_b16 v[186:187], v70 offset:2176\n\t" \
  "ds_read_b64_tr_b16 v[188:189], v70 offset:3072\n\t" \
  "ds_read_b64_tr_b16 v[190:191], v70 offset:3200\n\t" \
  "s_waitcnt lgkmcnt(0)\n\t" \
  "v_cmp_ne_u32 vcc, %[T], v225\n\t" \
  "s_cbranch_vccz 31f\n\t" \
  "s_sub_u32 %[b], %[b], 1\n\t" \
  "s_cbranch_scc0 30b\n\t" \
  "s_mov_b32 %[b], 0\n\t" \
  "s_branch 31f\n\t" \
  "40:\n\t" \
  "s_waitcnt lgkmcnt(0)\n\t" \
  "ds_read_b32 v225, v72 offset:2048\n\t" \
  "ds_read_b64_tr_b16 v[192:193], v70 offset:16\n\t" \
  "ds_read_b64_tr_b16 v[194:195], v70 offset:144\n\t" \
  "ds_read_b64_tr_b16 v[196:197], v70 offset:1040\n\t" \
  "ds_read_b64_tr_b16 v[198:199], v70 offset:1168\n\t" \
  "ds_read_b64_tr_b16 v[200:201], v70 offset:2064\n\t" \
  "ds_read_b64_tr_b16 v[202:203], v70 offset:2192\n\t" \
  "ds_read_b64_tr_b16 v[204:205], v70 offset:3088\n\t" \
  "ds_read_b64_tr_b16 v[206:207], v70 offset:3216\n\t" \
  "s_waitcnt lgkmcnt(8)\n\t" \
  "v_cmp_ne_u32 vcc, %[T], v225\n\t" \
  "s_cbranch_vccz 41f\n\t" \
  "s_sub_u32 %[b], %[b], 1\n\t" \
  "s_cbranch_scc0 40b\n\t" \
  "s_mov_b32 %[b], 0\n\t" \
  "s_branch 41f\n\t" \
  "12:\n\t" \
  "s_add_u32 %[f], %[f], 1\n\t" \
  "s_cmp_lt_u32 %[f], 0x200000\n\t" \
  "s_cbranch_scc0 11f\n\t" \
  "buffer_load_dwordx4 v[128:131], v64, %[d], %[o0] offen sc1\n\t" \
  "buffer_load_dwordx4 v[132:135], v64, %[d], %[o1] offen sc1\n\t" \
  "s_branch 10f\n\t" \
  "9:\n\t" \
  "buffer_load_dwordx4 v[128:131], v64, %[d], %[o0] offen sc1\n\t" \
  "buffer_load_dwordx4 v[132:135], v64, %[d], %[o1] offen sc1\n\t" \
  "v_mov_b64 v[160:161], 0\n\t" \
  "v_mov_b64 v[162:163], 0\n\t" \
  "v_mov_b64 v[164:165], 0\n\t" \
  "v_mov_b64 v[166:167], 0\n\t" \
  "v_mov_b64 v[168:169], 0\n\t" \
  "v_mov_b64 v[170:171], 0\n\t" \
  "v_mov_b64 v[172:173], 0\n\t" \
  "v_mov_b64 v[174:175], 0\n\t" \
  "v_add_u32 v244, %[xo], v112\n\t" \
  "v_add_u32 v245, %[xo], v113\n\t" \
  "v_add_u32 v246, %[xo], v114\n\t" \
  "v_add_u32 v247, %[xo], v115\n\t" \
  "v_mov_b32 v224, %[T]\n\t" \
  "v_add_u32 v227, %[kb], v64\n\t" \
  "ds_read_b128 v[80:83], v227 offset:0\n\t" \
  "ds_read_b128 v[84:87], v227 offset:1024\n\t" \
  "ds_read_b128 v[88:91], v227 offset:2048\n\t" \
  "ds_read_b128 v[92:95], v227 offset:3072\n\t" \
  "ds_read_b128 v[96:99], v227 offset:4096\n\t" \
  "ds_read_b128 v[100:103], v227 offset:5120\n\t" \
  "ds_read_b128 v[104:107], v227 offset:6144\n\t" \
  "ds_read_b128 v[108:111], v227 offset:7168\n\t" \
  "10:\n\t" \
  "s_waitcnt vmcnt(1)\n\t" \
  "ds_write_b128 v244, v[128:131]\n\t" \
  "v_max3_u32 v226, v128, v129, v130\n\t" \
  "s_waitcnt vmcnt(0)\n\t" \
  "ds_write_b128 v244, v[132:135] offset:4096\n\t" \
  "v_max3_u32 v227, v132, v133, v134\n\t" \
  "v_max3_u32 v226, v226, v131, v135\n\t" \
  "v_max_u32 v226, v226, v227\n\t" \
  "v_cmp_eq_u32 vcc, -1, v226\n\t" \
  "s_cbranch_vccnz 12b\n\t" \
  "11:\n\t" \
  "v_smfmac_f32_16x16x64_bf16 v[160:163], v[128:131], a[0:7], v73\n\t" \
  "v_smfmac_f32_16x16x64_bf16 v[164:167], v[128:131], a[64:71], v73\n\t" \
  "v_smfmac_f32_16x16x64_bf16 v[160:163], v[132:135], a[32:39], v73\n\t" \
  "s_waitcnt lgkmcnt(0)\n\t" \
  "s_barrier\n\t" \
  "ds_read_b128 v[136:139], v245\n\t" \
  "ds_read_b128 v[140:143], v245 offset:4096\n\t" \
  "ds_read_b128 v[144:147], v246\n\t" \
  "ds_read_b128 v[148:151], v246 offset:4096\n\t" \
  "ds_read_b128 v[152:155], v247\n\t" \
  "ds_read_b128 v[156:159], v247 offset:4096\n\t" \
  "v_smfmac_f32_16x16x64_bf16 v[164:167], v[132:135], a[96:103], v73\n\t" \
  "v_smfmac_f32_16x16x64_bf16 v[168:171], v[128:131], a[128:135], v73\n\t" \
  "v_smfmac_f32_16x16x64_bf16 v[172:175], v[128:131], a[192:199], v73\n\t" \
  "v_smfmac_f32_16x16x64_bf16 v[168:171], v[132:135], a[160:167], v73\n\t" \
  "v_smfmac_f32_16x16x64_bf16 v[172:175], v[132:135], a[224:231], v73\n\t" \
  "21:\n\t" \
  "s_waitcnt lgkmcnt(5)\n\t" \
  "v_smfmac_f32_16x16x64_bf16 v[160:163], v[136:139], a[8:15], v73\n\t" \
  "v_smfmac_f32_16x16x64_bf16 v[164:167], v[136:139], a[72:79], v73\n\t" \
  "s_waitcnt lgkmcnt(4)\n\t" \
  "v_smfmac_f32_16x16x64_bf16 v[160:163], v[140:143], a[40:47], v73\n\t" \
  "v_smfmac_f32_16x16x64_bf16 v[164:167], v[140:143], a[104:111], v73\n\t" \
  "s_waitcnt lgkmcnt(3)\n\t" \
  "v_smfmac_f32_16x16x64_bf16 v[160:163], v[144:147], a[16:23], v73\n\t" \
  "v_smfmac_f32_16x16x64_bf16 v[164:167], v[144:147], a[80:87], v73\n\t" \
  "s_waitcnt lgkmcnt(2)\n\t" \
  "v_smfmac_f32_16x16x64_bf16 v[160:163], v[148:151], a[48:55], v73\n\t" \
  "v_smfmac_f32_16x16x64_bf16 v[164:167], v[148:151], a[112:119], v73\n\t" \
  "s_waitcnt lgkmcnt(1)\n\t" \
  "v_smfmac_f32_16x16x64_bf16 v[160:163], v[152:155], a[24:31], v73\n\t" \
  "v_smfmac_f32_16x16x64_bf16 v[164:167], v[152:155], a[88:95], v73\n\t" \
  "s_waitcnt lgkmcnt(0)\n\t" \
  "v_smfmac_f32_16x16x64_bf16 v[160:163], v[156:159], a[56:63], v73\n\t" \
  "v_smfmac_f32_16x16x64_bf16 v[164:167], v[156:159], a[120:127], v73\n\t" \
  "v_smfmac_f32_16x16x64_bf16 v[168:171], v[136:139], a[136:143], v73\n\t" \
  "v_smfmac_f32_16x16x64_bf16 v[172:175], v[136:139], a[200:207], v73\n\t" \
  "v_smfmac_f32_16x16x64_bf16 v[168:171], v[140:143], a[168:175], v73\n\t" \
  "v_permlane32_swap_b32 v160, v164\n\t" \
  "v_permlane32_swap_b32 v161, v165\n\t" \
  "v_smfmac_f32_16x16x64_bf16 v[172:175], v[140:143], a[232:239], v73\n\t" \
  "v_permlane32_swap_b32 v162, v166\n\t" \
  "v_permlane32_swap_b32 v163, v167\n\t" \
  "v_add_f32 v160, v160, v164\n\t" \
  "v_smfmac_f32_16x16x64_bf16 v[168:171], v[144:147], a[144:151], v73\n\t" \
  "v_add_f32 v161, v161, v165\n\t" \
  "v_add_f32 v162, v162, v166\n\t" \
  "v_add_f32 v163, v163, v167\n\t" \
  "v_smfmac_f32_16x16x64_bf16 v[172:175], v[144:147], a[208:215], v73\n\t" \
  "v_cvt_pk_bf16_f32 v160, v160, v161\n\t" \
  "v_cvt_pk_bf16_f32 v161, v162, v163\n\t" \
  "ds_write_b64 v69, v[160:161]\n\t" \
  "v_smfmac_f32_16x16x64_bf16 v[168:171], v[148:151], a[176:183], v73\n\t" \
  "ds_write_b32 v71, v224 offset:1024\n\t" \
  "v_smfmac_f32_16x16x64_bf16 v[172:175], v[148:151], a[240:247], v73\n\t" \
  "v_smfmac_f32_16x16x64_bf16 v[168:171], v[152:155], a[152:159], v73\n\t" \
  "v_smfmac_f32_16x16x64_bf16 v[172:175], v[152:155], a[216:223], v73\n\t" \
  "v_smfmac_f32_16x16x64_bf16 v[168:171], v[156:159], a[184:191], v73\n\t" \
  "v_smfmac_f32_16x16x64_bf16 v[172:175], v[156:159], a[248:255], v73\n\t" \
  "ds_read_b32 v225, v72 offset:1024\n\t" \
  "ds_read_b64_tr_b16 v[176:177], v70 offset:0\n\t" \
  "ds_read_b64_tr_b16 v[178:179], v70 offset:128\n\t" \
  "ds_read_b64_tr_b16 v[180:181], v70 offset:1024\n\t" \
  "ds_read_b64_tr_b16 v[182:183], v70 offset:1152\n\t" \
  "ds_read_b64_tr_b16 v[184:185], v70 offset:2048\n\t" \
  "ds_read_b64_tr_b16 v[186:187], v70 offset:2176\n\t" \
  "ds_read_b64_tr_b16 v[188:189], v70 offset:3072\n\t" \
  "ds_read_b64_tr_b16 v[190:191], v70 offset:3200\n\t" \
  "v_permlane32_swap_b32 v168, v172\n\t" \
  "v_permlane32_swap_b32 v169, v173\n\t" \
  "v_permlane32_swap_b32 v170, v174\n\t" \
  "v_permlane32_swap_b32 v171, v175\n\t" \
  "v_add_f32 v168, v168, v172\n\t" \
  "v_add_f32 v169, v169, v173\n\t" \
  "v_add_f32 v170, v170, v174\n\t" \
  "v_cvt_pk_bf16_f32 v168, v168, v169\n\t" \
  "v_add_f32 v171, v171, v175\n\t" \
  "v_cvt_pk_bf16_f32 v169, v170, v171\n\t" \
  "ds_write_b64 v69, v[168:169] offset:16\n\t" \
  "ds_write_b32 v71, v224 offset:2048\n\t" \
  "s_waitcnt lgkmcnt(2)\n\t" \
  "v_cmp_ne_u32 vcc, %[T], v225\n\t" \
  "s_cbranch_vccnz 30b\n\t" \
  "31:\n\t" \
  "v_mfma_f32_16x16x32_bf16 v[208:211], v[80:83], v[176:179], 0\n\t" \
  "v_mfma_f32_16x16x32_bf16 v[212:215], v[96:99], v[176:179], 0\n\t" \
  "ds_read_b32 v225, v72 offset:2048\n\t" \
  "ds_read_b64_tr_b16 v[192:193], v70 offset:16\n\t" \
  "ds_read_b64_tr_b16 v[194:195], v70 offset:144\n\t" \
  "v_mfma_f32_16x16x32_bf16 v[208:211], v[84:87], v[180:183], v[208:211]\n\t" \
  "ds_read_b64_tr_b16 v[196:197], v70 offset:1040\n\t" \
  "ds_read_b64_tr_b16 v[198:199], v70 offset:1168\n\t" \
  "v_mfma_f32_16x16x32_bf16 v[212:215], v[100:103], v[180:183], v[212:215]\n\t" \
  "ds_read_b64_tr_b16 v[200:201], v70 offset:2064\n\t" \
  "ds_read_b64_tr_b16 v[202:203], v70 offset:2192\n\t" \
  "v_mfma_f32_16x16x32_bf16 v[208:211], v[88:91], v[184:187], v[208:211]\n\t" \
  "ds_read_b64_tr_b16 v[204:205], v70 offset:3088\n\t" \
  "ds_read_b64_tr_b16 v[206:207], v70 offset:3216\n\t" \
  "v_mfma_f32_16x16x32_bf16 v[212:215], v[104:107], v[184:187], v[212:215]\n\t" \
  "v_mfma_f32_16x16x32_bf16 v[208:211], v[92:95], v[188:191], v[208:211]\n\t" \
  "v_mfma_f32_16x16x32_bf16 v[212:215], v[108:111], v[188:191], v[212:215]\n\t" \
  "s_nop 4\n\t" \
  "s_waitcnt lgkmcnt(8)\n\t" \
  "v_cmp_ne_u32 vcc, %[T], v225\n\t" \
  "s_cbranch_vccnz 40b\n\t" \
  "41:\n\t" \
  "v_cndmask_b32 v220, v208, v212, %[hi]\n\t" \
  "v_cndmask_b32 v221, v209, v213, %[hi]\n\t" \
  "v_cndmask_b32 v222, v210, v214, %[hi]\n\t" \
  "v_cndmask_b32 v223, v211, v215, %[hi]\n\t" \
  "v_max_f32 v226, v220, v221\n\t" \
  "v_max3_f32 v226, v226, v222, v223\n\t" \
  "s_waitcnt lgkmcnt(6)\n\t" \
  "v_mfma_f32_16x16x32_bf16 v[216:219], v[192:195], v[176:179], 0\n\t" \
  "v_max_f32_dpp v228, v226, v226 row_ror:8 row_mask:0xf bank_mask:0xf\n\t" \
  "v_mov_b32 v227, v228\n\t" \
  "s_waitcnt lgkmcnt(4)\n\t" \
  "v_mfma_f32_16x16x32_bf16 v[216:219], v[196:199], v[180:183], v[216:219]\n\t" \
  "v_permlane16_swap_b32 v228, v227\n\t" \
  "v_max_f32 v228, v228, v227\n\t" \
  "v_mov_b32 v227, v228\n\t" \
  "s_waitcnt lgkmcnt(2)\n\t" \
  "v_mfma_f32_16x16x32_bf16 v[216:219], v[200:203], v[184:187], v[216:219]\n\t" \
  "v_permlane32_swap_b32 v228, v227\n\t" \
  "s_waitcnt lgkmcnt(0)\n\t" \
  "v_mfma_f32_16x16x32_bf16 v[216:219], v[204:207], v[188:191], v[216:219]\n\t" \
  "v_max_f32 v228, v228, v227\n\t" \
  "v_mul_f32 v229, %[sc], v228\n\t" \
  "v_fma_f32 v220, v220, %[sc], -v229\n\t" \
  "v_fma_f32 v221, v221, %[sc], -v229\n\t" \
  "v_fma_f32 v222, v222, %[sc], -v229\n\t" \
  "v_fma_f32 v223, v223, %[sc], -v229\n\t" \
  "v_exp_f32 v220, v220\n\t" \
  "v_exp_f32 v221, v221\n\t" \
  "v_exp_f32 v222, v222\n\t" \
  "v_exp_f32 v223, v223\n\t" \
  "v_max3_f32 v230, v216, v217, v218\n\t" \
  "v_add_f32 v232, v220, v221\n\t" \
  "v_max3_f32 v230, v230, v219, v228\n\t" \
  "v_add_f32 v234, v222, v223\n\t" \
  "v_mov_b32 v231, v230\n\t" \
  "v_add_f32 v234, v232, v234\n\t" \
  "v_cndmask_b32 v238, v216, v218, %[hi]\n\t" \
  "v_permlane16_swap_b32 v230, v231\n\t" \
  "v_add_f32_dpp v232, v234, v234 row_ror:8 row_mask:0xf bank_mask:0xf\n\t" \
  "v_max_f32 v230, v230, v231\n\t" \
  "v_mov_b32 v233, v232\n\t" \
  "v_mul_f32 v235, %[nsc], v230\n\t" \
  "v_cndmask_b32 v239, v217, v219, %[hi]\n\t" \
  "v_permlane16_swap_b32 v232, v233\n\t" \
  "v_fma_f32 v238, v238, %[sc], v235\n\t" \
  "v_fma_f32 v239, v239, %[sc], v235\n\t" \
  "v_add_f32 v232, v232, v233\n\t" \
  "v_exp_f32 v238, v238\n\t" \
  "v_mov_b32 v233, v232\n\t" \
  "v_exp_f32 v239, v239\n\t" \
  "v_add_f32 v236, v229, v235\n\t" \
  "v_permlane32_swap_b32 v232, v233\n\t" \
  "v_add_f32 v240, v238, v239\n\t" \
  "v_exp_f32 v236, v236\n\t" \
  "v_add_f32 v232, v232, v233\n\t" \
  "v_add_f32_dpp v241, v240, v240 row_ror:8 row_mask:0xf bank_mask:0xf\n\t" \
  "v_mov_b32 v240, v241\n\t" \
  "v_pk_mul_f32 v[220:221], v[220:221], v[236:237] op_sel_hi:[1,0]\n\t" \
  "v_pk_mul_f32 v[222:223], v[222:223], v[236:237] op_sel_hi:[1,0]\n\t" \
  "v_permlane16_swap_b32 v240, v241\n\t" \
  "v_fma_f32 v232, v232, v236, v240\n\t" \
  "v_add_f32 v232, v232, v241\n\t" \
  "v_rcp_f32 v242, v232\n\t" \
  "s_nop 0\n\t" \
  "v_pk_mul_f32 v[220:221], v[220:221], v[242:243] op_sel_hi:[1,0]\n\t" \
  "v_pk_mul_f32 v[222:223], v[222:223], v[242:243] op_sel_hi:[1,0]\n\t" \
  "v_pk_mul_f32 v[238:239], v[238:239], v[242:243] op_sel_hi:[1,0]\n\t" \
  "v_cvt_pk_bf16_f32 v220, v220, v221\n\t" \
  "v_cvt_pk_bf16_f32 v221, v222, v223\n\t" \
  "v_cvt_pk_bf16_f32 v238, v238, v239\n\t" \
  A_STORE \
  "99:\n\t" \
  ""
#define CLOB_A0 "v80", "v81", "v82", "v83", "v84", "v85", "v86", "v87", "v88", "v89", "v90", "v91", "v92", "v93", "v94", "v95", "v96", "v97", "v98", "v99", "v100", "v101", "v102", "v103", "v104", "v105", "v106", "v107", "v108", "v109", "v110", "v111", "v128", "v129", "v130", "v131", "v132", "v133", "v134", "v135", "v136", "v137", "v138", "v139", "v140", "v141", "v142", "v143", "v144", "v145", "v146", "v147", "v148", "v149", "v150", "v151", "v152", "v153", "v154", "v155", "v156", "v157", "v158", "v159", "v160", "v161", "v162", "v163", "v164", "v165", "v166", "v167", "v168", "v169", "v170", "v171", "v172", "v173", "v174", "v175", "v176", "v177", "v178", "v179", "v180", "v181", "v182", "v183", "v184", "v185", "v186", "v187", "v188", "v189", "v190", "v191", "v192", "v193", "v194", "v195", "v196", "v197", "v198", "v199", "v200", "v201", "v202", "v203", "v204", "v205", "v206", "v207", "v208", "v209", "v210", "v211", "v212", "v213", "v214", "v215", "v216", "v217", "v218", "v219", "v220", "v221", "v222", "v223", "v224", "v225", "v226", "v227", "v228", "v229", "v230", "v231", "v232", "v233", "v234", "v235", "v236", "v237", "v238", "v239", "v240", "v241", "v242", "v243", "v244", "v245", "v246", "v247"
#define ASM_A1 \
  "s_branch 9f\n\t" \
  "12:\n\t" \
  "s_add_u32 %[f], %[f], 1\n\t" \
  "s_cmp_lt_u32 %[f], 0x200000\n\t" \
  "s_cbranch_scc0 11f\n\t" \
  "buffer_load_dwordx4 v[128:131], v64, %[d], %[o0] offen sc1\n\t" \
  "buffer_load_dwordx4 v[132:135], v64, %[d], %[o1] offen sc1\n\t" \
  "s_branch 10f\n\t" \
  "9:\n\t" \
  "buffer_load_dwordx4 v[128:131], v64, %[d], %[o0] offen sc1\n\t" \
  "buffer_load_dwordx4 v[132:135], v64, %[d], %[o1] offen sc1\n\t" \
  "v_mov_b64 v[160:161], 0\n\t" \
  "v_mov_b64 v[162:163], 0\n\t" \
  "v_mov_b64 v[164:165], 0\n\t" \
  "v_mov_b64 v[166:167], 0\n\t" \
  "v_mov_b64 v[168:169], 0\n\t" \
  "v_mov_b64 v[170:171], 0\n\t" \
  "v_mov_b64 v[172:173], 0\n\t" \
  "v_mov_b64 v[174:175], 0\n\t" \
  "v_add_u32 v244, %[xo], v112\n\t" \
  "v_add_u32 v245, %[xo], v113\n\t" \
  "v_add_u32 v246, %[xo], v114\n\t" \
  "v_add_u32 v247, %[xo], v115\n\t" \
  "v_mov_b32 v224, %[T]\n\t" \
  "10:\n\t" \
  "s_waitcnt vmcnt(1)\n\t" \
  "ds_write_b128 v244, v[128:131]\n\t" \
  "v_max3_u32 v226, v128, v129, v130\n\t" \
  "s_waitcnt vmcnt(0)\n\t" \
  "ds_write_b128 v244, v[132:135] offset:4096\n\t" \
  "v_max3_u32 v227, v132, v133, v134\n\t" \
  "v_max3_u32 v226, v226, v131, v135\n\t" \
  "v_max_u32 v226, v226, v227\n\t" \
  "v_cmp_eq_u32 vcc, -1, v226\n\t" \
  "s_cbranch_vccnz 12b\n\t" \
  "11:\n\t" \
  "v_smfmac_f32_16x16x64_bf16 v[160:163], v[128:131], a[0:7], v73\n\t" \
  "v_smfmac_f32_16x16x64_bf16 v[164:167], v[128:131], a[64:71], v73\n\t" \
  "v_smfmac_f32_16x16x64_bf16 v[160:163], v[132:135], a[32:39], v73\n\t" \
  "s_waitcnt lgkmcnt(0)\n\t" \
  "s_barrier\n\t" \
  "ds_read_b128 v[136:139], v245\n\t" \
  "ds_read_b128 v[140:143], v245 offset:4096\n\t" \
  "ds_read_b128 v[144:147], v246\n\t" \
  "ds_read_b128 v[148:151], v246 offset:4096\n\t" \
  "ds_read_b128 v[152:155], v247\n\t" \
  "ds_read_b128 v[156:159], v247 offset:4096\n\t" \
  "v_smfmac_f32_16x16x64_bf16 v[164:167], v[132:135], a[96:103], v73\n\t" \
  "v_smfmac_f32_16x16x64_bf16 v[168:171], v[128:131], a[128:135], v73\n\t" \
  "v_smfmac_f32_16x16x64_bf16 v[172:175], v[128:131], a[192:199], v73\n\t" \
  "v_smfmac_f32_16x16x64_bf16 v[168:171], v[132:135], a[160:167], v73\n\t" \
  "v_smfmac_f32_16x16x64_bf16 v[172:175], v[132:135], a[224:231], v73\n\t" \
  "21:\n\t" \
  "s_waitcnt lgkmcnt(5)\n\t" \
  "v_smfmac_f32_16x16x64_bf16 v[160:163], v[136:139], a[8:15], v73\n\t" \
  "v_smfmac_f32_16x16x64_bf16 v[164:167], v[136:139], a[72:79], v73\n\t" \
  "s_waitcnt lgkmcnt(4)\n\t" \
  "v_smfmac_f32_16x16x64_bf16 v[160:163], v[140:143], a[40:47], v73\n\t" \
  "v_smfmac_f32_16x16x64_bf16 v[164:167], v[140:143], a[104:111], v73\n\t" \
  "s_waitcnt lgkmcnt(3)\n\t" \
  "v_smfmac_f32_16x16x64_bf16 v[160:163], v[144:147], a[16:23], v73\n\t" \
  "v_smfmac_f32_16x16x64_bf16 v[164:167], v[144:147], a[80:87], v73\n\t" \
  "s_waitcnt lgkmcnt(2)\n\t" \
  "v_smfmac_f32_16x16x64_bf16 v[160:163], v[148:151], a[48:55], v73\n\t" \
  "v_smfmac_f32_16x16x64_bf16 v[164:167], v[148:151], a[112:119], v73\n\t" \
  "s_waitcnt lgkmcnt(1)\n\t" \
  "v_smfmac_f32_16x16x64_bf16 v[160:163], v[152:155], a[24:31], v73\n\t" \
  "v_smfmac_f32_16x16x64_bf16 v[164:167], v[152:155], a[88:95], v73\n\t" \
  "s_waitcnt lgkmcnt(0)\n\t" \
  "v_smfmac_f32_16x16x64_bf16 v[160:163], v[156:159], a[56:63], v73\n\t" \
  "v_smfmac_f32_16x16x64_bf16 v[164:167], v[156:159], a[120:127], v73\n\t" \
  "v_smfmac_f32_16x16x64_bf16 v[168:171], v[136:139], a[136:143], v73\n\t" \
  "v_smfmac_f32_16x16x64_bf16 v[172:175], v[136:139], a[200:207], v73\n\t" \
  "v_smfmac_f32_16x16x64_bf16 v[168:171], v[140:143], a[168:175], v73\n\t" \
  "v_permlane32_swap_b32 v160, v164\n\t" \
  "v_permlane32_swap_b32 v161, v165\n\t" \
  "v_smfmac_f32_16x16x64_bf16 v[172:175], v[140:143], a[232:239], v73\n\t" \
  "v_permlane32_swap_b32 v162, v166\n\t" \
  "v_permlane32_swap_b32 v163, v167\n\t" \
  "v_add_f32 v160, v160, v164\n\t" \
  "v_smfmac_f32_16x16x64_bf16 v[168:171], v[144:147], a[144:151], v73\n\t" \
  "v_add_f32 v161, v161, v165\n\t" \
  "v_add_f32 v162, v162, v166\n\t" \
  "v_add_f32 v163, v163, v167\n\t" \
  "v_smfmac_f32_16x16x64_bf16 v[172:175], v[144:147], a[208:215], v73\n\t" \
  "v_cvt_pk_bf16_f32 v160, v160, v161\n\t" \
  "v_cvt_pk_bf16_f32 v161, v162, v163\n\t" \
  "ds_write_b64 v69, v[160:161]\n\t" \
  "v_smfmac_f32_16x16x64_bf16 v[168:171], v[148:151], a[176:183], v73\n\t" \
  "ds_write_b32 v71, v224 offset:1024\n\t" \
  "v_smfmac_f32_16x16x64_bf16 v[172:175], v[148:151], a[240:247], v73\n\t" \
  "v_smfmac_f32_16x16x64_bf16 v[168:171], v[152:155], a[152:159], v73\n\t" \
  "v_smfmac_f32_16x16x64_bf16 v[172:175], v[152:155], a[216:223], v73\n\t" \
  "v_smfmac_f32_16x16x64_bf16 v[168:171], v[156:159], a[184:191], v73\n\t" \
  "v_smfmac_f32_16x16x64_bf16 v[172:175], v[156:159], a[248:255], v73\n\t" \
  "s_nop 7\n\t" \
  "v_permlane32_swap_b32 v168, v172\n\t" \
  "v_permlane32_swap_b32 v169, v173\n\t" \
  "v_permlane32_swap_b32 v170, v174\n\t" \
  "v_permlane32_swap_b32 v171, v175\n\t" \
  "v_add_f32 v168, v168, v172\n\t" \
  "v_add_f32 v169, v169, v173\n\t" \
  "v_add_f32 v170, v170, v174\n\t" \
  "v_cvt_pk_bf16_f32 v168, v168, v169\n\t" \
  "v_add_f32 v171, v171, v175\n\t" \
  "v_cvt_pk_bf16_f32 v169, v170, v171\n\t" \
  "ds_write_b64 v69, v[168:169] offset:16\n\t" \
  "ds_write_b32 v71, v224 offset:2048\n\t" \
  "99:\n\t" \
  ""
#define CLOB_A1 "v128", "v129", "v130", "v131", "v132", "v133", "v134", "v135", "v136", "v137", "v138", "v139", "v140", "v141", "v142", "v143", "v144", "v145", "v146", "v147", "v148", "v149", "v150", "v151", "v152", "v153", "v154", "v155", "v156", "v157", "v158", "v159", "v160", "v161", "v162", "v163", "v164", "v165", "v166", "v167", "v168", "v169", "v170", "v171", "v172", "v173", "v174", "v175", "v224", "v226", "v227", "v244", "v245", "v246", "v247"
#define ASM_V \
  "s_branch 9f\n\t" \
  "30:\n\t" \
  "s_waitcnt lgkmcnt(0)\n\t" \
  "ds_read_b32 v225, v72 offset:1024\n\t" \
  "ds_read_b64_tr_b16 v[180:181], v77 offset:0\n\t" \
  "ds_read_b64_tr_b16 v[182:183], v77 offset:128\n\t" \
  "ds_read_b64_tr_b16 v[184:185], v78 offset:0\n\t" \
  "ds_read_b64_tr_b16 v[186:187], v78 offset:128\n\t" \
  "ds_read_b64_tr_b16 v[188:189], v79 offset:0\n\t" \
  "ds_read_b64_tr_b16 v[190:191], v79 offset:128\n\t" \
  "s_waitcnt lgkmcnt(6)\n\t" \
  "v_cmp_ne_u32 vcc, %[T], v225\n\t" \
  "s_cbranch_vccz 31f\n\t" \
  "s_sub_u32 %[b], %[b], 1\n\t" \
  "s_cbranch_scc0 30b\n\t" \
  "s_mov_b32 %[b], 0\n\t" \
  "s_branch 31f\n\t" \
  "12:\n\t" \
  "s_add_u32 %[f], %[f], 1\n\t" \
  "s_cmp_lt_u32 %[f], 0x200000\n\t" \
  "s_cbranch_scc0 11f\n\t" \
  "buffer_load_dwordx4 v[128:131], v64, %[d], %[o0] offen sc1\n\t" \
  "buffer_load_dwordx4 v[132:135], v64, %[d], %[o1] offen sc1\n\t" \
  "s_branch 10f\n\t" \
  "9:\n\t" \
  "v_mov_b64 v[160:161], 0\n\t" \
  "v_mov_b64 v[162:163], 0\n\t" \
  "v_mov_b64 v[164:165], 0\n\t" \
  "v_mov_b64 v[166:167], 0\n\t" \
  "v_mov_b64 v[168:169], 0\n\t" \
  "v_mov_b64 v[170:171], 0\n\t" \
  "v_mov_b64 v[172:173], 0\n\t" \
  "v_mov_b64 v[174:175], 0\n\t" \
  "v_mov_b64 v[208:209], 0\n\t" \
  "v_mov_b64 v[210:211], 0\n\t" \
  "v_mov_b64 v[212:213], 0\n\t" \
  "v_mov_b64 v[214:215], 0\n\t" \
  "v_add_u32 v244, %[xo], v112\n\t" \
  "v_add_u32 v245, %[xo], v113\n\t" \
  "v_add_u32 v246, %[xo], v114\n\t" \
  "v_add_u32 v247, %[xo], v115\n\t" \
  "v_mov_b32 v224, %[T]\n\t" \
  "buffer_load_dwordx4 v[128:131], v64, %[d], %[o0] offen sc1\n\t" \
  "buffer_load_dwordx4 v[132:135], v64, %[d], %[o1] offen sc1\n\t" \
  "10:\n\t" \
  "s_waitcnt vmcnt(1)\n\t" \
  "ds_write_b128 v244, v[128:131]\n\t" \
  "v_max3_u32 v226, v128, v129, v130\n\t" \
  "s_waitcnt vmcnt(0)\n\t" \
  "ds_write_b128 v244, v[132:135] offset:4096\n\t" \
  "v_max3_u32 v227, v132, v133, v134\n\t" \
  "v_max3_u32 v226, v226, v131, v135\n\t" \
  "v_max_u32 v226, v226, v227\n\t" \
  "v_cmp_eq_u32 vcc, -1, v226\n\t" \
  "s_cbranch_vccnz 12b\n\t" \
  "11:\n\t" \
  "v_smfmac_f32_16x16x64_bf16 v[160:163], v[128:131], a[0:7], v73\n\t" \
  "v_smfmac_f32_16x16x64_bf16 v[164:167], v[128:131], a[64:71], v73\n\t" \
  "s_waitcnt lgkmcnt(0)\n\t" \
  "s_barrier\n\t" \
  "ds_read_b128 v[136:139], v245\n\t" \
  "ds_read_b128 v[140:143], v245 offset:4096\n\t" \
  "ds_read_b128 v[144:147], v246\n\t" \
  "ds_read_b128 v[148:151], v246 offset:4096\n\t" \
  "ds_read_b128 v[152:155], v247\n\t" \
  "ds_read_b128 v[156:159], v247 offset:4096\n\t" \
  "v_smfmac_f32_16x16x64_bf16 v[160:163], v[132:135], a[32:39], v73\n\t" \
  "v_smfmac_f32_16x16x64_bf16 v[164:167], v[132:135], a[96:103], v73\n\t" \
  "21:\n\t" \
  "s_waitcnt lgkmcnt(5)\n\t" \
  "v_smfmac_f32_16x16x64_bf16 v[160:163], v[136:139], a[8:15], v73\n\t" \
  "v_smfmac_f32_16x16x64_bf16 v[164:167], v[136:139], a[72:79], v73\n\t" \
  "s_waitcnt lgkmcnt(4)\n\t" \
  "v_smfmac_f32_16x16x64_bf16 v[160:163], v[140:143], a[40:47], v73\n\t" \
  "v_smfmac_f32_16x16x64_bf16 v[164:167], v[140:143], a[104:111], v73\n\t" \
  "s_waitcnt lgkmcnt(3)\n\t" \
  "v_smfmac_f32_16x16x64_bf16 v[160:163], v[144:147], a[16:23], v73\n\t" \
  "v_smfmac_f32_16x16x64_bf16 v[164:167], v[144:147], a[80:87], v73\n\t" \
  "s_waitcnt lgkmcnt(2)\n\t" \
  "v_smfmac_f32_16x16x64_bf16 v[160:163], v[148:151], a[48:55], v73\n\t" \
  "v_smfmac_f32_16x16x64_bf16 v[164:167], v[148:151], a[112:119], v73\n\t" \
  "s_waitcnt lgkmcnt(1)\n\t" \
  "v_smfmac_f32_16x16x64_bf16 v[160:163], v[152:155], a[24:31], v73\n\t" \
  "v_smfmac_f32_16x16x64_bf16 v[164:167], v[152:155], a[88:95], v73\n\t" \
  "s_waitcnt lgkmcnt(0)\n\t" \
  "v_smfmac_f32_16x16x64_bf16 v[160:163], v[156:159], a[56:63], v73\n\t" \
  "v_smfmac_f32_16x16x64_bf16 v[164:167], v[156:159], a[120:127], v73\n\t" \
  "s_nop 7\n\t" \
  "v_permlane32_swap_b32 v160, v164\n\t" \
  "v_permlane32_swap_b32 v161, v165\n\t" \
  "v_permlane32_swap_b32 v162, v166\n\t" \
  "v_permlane32_swap_b32 v163, v167\n\t" \
  "v_add_f32 v160, v160, v164\n\t" \
  "v_add_f32 v161, v161, v165\n\t" \
  "v_add_f32 v162, v162, v166\n\t" \
  "v_add_f32 v163, v163, v167\n\t" \
  "v_cvt_pk_bf16_f32 v160, v160, v161\n\t" \
  "v_cvt_pk_bf16_f32 v161, v162, v163\n\t" \
  "ds_write_b64 v69, v[160:161]\n\t" \
  "ds_write_b32 v71, v224 offset:1024\n\t" \
  "ds_read_b64_tr_b16 v[176:177], v76 offset:0\n\t" \
  "ds_read_b64_tr_b16 v[178:179], v76 offset:128\n\t" \
  "ds_read_b32 v225, v72 offset:1024\n\t" \
  "ds_read_b64_tr_b16 v[180:181], v77 offset:0\n\t" \
  "ds_read_b64_tr_b16 v[182:183], v77 offset:128\n\t" \
  "ds_read_b64_tr_b16 v[184:185], v78 offset:0\n\t" \
  "ds_read_b64_tr_b16 v[186:187], v78 offset:128\n\t" \
  "ds_read_b64_tr_b16 v[188:189], v79 offset:0\n\t" \
  "ds_read_b64_tr_b16 v[190:191], v79 offset:128\n\t" \
  "s_waitcnt lgkmcnt(7)\n\t" \
  "v_smfmac_f32_16x16x64_bf16 v[168:171], v[176:179], a[128:135], v73\n\t" \
  "v_smfmac_f32_16x16x64_bf16 v[172:175], v[176:179], a[136:143], v73\n\t" \
  "v_smfmac_f32_16x16x64_bf16 v[208:211], v[176:179], a[144:151], v73\n\t" \
  "s_waitcnt lgkmcnt(6)\n\t" \
  "v_cmp_ne_u32 vcc, %[T], v225\n\t" \
  "v_smfmac_f32_16x16x64_bf16 v[212:215], v[176:179], a[152:159], v73\n\t" \
  "s_cbranch_vccnz 30b\n\t" \
  "31:\n\t" \
  "s_waitcnt lgkmcnt(4)\n\t" \
  "v_smfmac_f32_16x16x64_bf16 v[168:171], v[180:183], a[160:167], v73\n\t" \
  "v_smfmac_f32_16x16x64_bf16 v[172:175], v[180:183], a[168:175], v73\n\t" \
  "v_smfmac_f32_16x16x64_bf16 v[208:211], v[180:183], a[176:183], v73\n\t" \
  "v_smfmac_f32_16x16x64_bf16 v[212:215], v[180:183], a[184:191], v73\n\t" \
  "s_waitcnt lgkmcnt(2)\n\t" \
  "v_smfmac_f32_16x16x64_bf16 v[168:171], v[184:187], a[192:199], v73\n\t" \
  "v_smfmac_f32_16x16x64_bf16 v[172:175], v[184:187], a[200:207], v73\n\t" \
  "v_smfmac_f32_16x16x64_bf16 v[208:211], v[184:187], a[208:215], v73\n\t" \
  "v_smfmac_f32_16x16x64_bf16 v[212:215], v[184:187], a[216:223], v73\n\t" \
  "s_waitcnt lgkmcnt(0)\n\t" \
  "v_smfmac_f32_16x16x64_bf16 v[168:171], v[188:191], a[224:231], v73\n\t" \
  "v_smfmac_f32_16x16x64_bf16 v[172:175], v[188:191], a[232:239], v73\n\t" \
  "v_smfmac_f32_16x16x64_bf16 v[208:211], v[188:191], a[240:247], v73\n\t" \
  "v_smfmac_f32_16x16x64_bf16 v[212:215], v[188:191], a[248:255], v73\n\t" \
  "s_nop 0\n\t" \
  "v_cvt_pk_bf16_f32 v220, v168, v169\n\t" \
  "v_cvt_pk_bf16_f32 v221, v170, v171\n\t" \
  "s_nop 1\n\t" \
  "v_cvt_pk_bf16_f32 v222, v172, v173\n\t" \
  "v_cvt_pk_bf16_f32 v223, v174, v175\n\t" \
  "v_min_u32 v220, -2, v220\n\t" \
  "v_min_u32 v221, -2, v221\n\t" \
  "v_min_u32 v222, -2, v222\n\t" \
  "v_min_u32 v223, -2, v223\n\t" \
  "v_cvt_pk_bf16_f32 v228, v208, v209\n\t" \
  "v_cvt_pk_bf16_f32 v229, v210, v211\n\t" \
  "v_permlane16_swap_b32 v220, v222\n\t" \
  "v_permlane16_swap_b32 v221, v223\n\t" \
  "v_cvt_pk_bf16_f32 v230, v212, v213\n\t" \
  "v_cvt_pk_bf16_f32 v231, v214, v215\n\t" \
  V_STORE0 \
  "v_min_u32 v228, -2, v228\n\t" \
  "v_min_u32 v229, -2, v229\n\t" \
  "v_min_u32 v230, -2, v230\n\t" \
  "v_min_u32 v231, -2, v231\n\t" \
  "s_nop 1\n\t" \
  "v_permlane16_swap_b32 v228, v230\n\t" \
  "v_permlane16_swap_b32 v229, v231\n\t" \
  V_STORE1 \
  ""
#define CLOB_V "v128", "v129", "v130", "v131", "v132", "v133", "v134", "v135", "v136", "v137", "v138", "v139", "v140", "v141", "v142", "v143", "v144", "v145", "v146", "v147", "v148", "v149", "v150", "v151", "v152", "v153", "v154", "v155", "v156", "v157", "v158", "v159", "v160", "v161", "v162", "v163", "v164", "v165", "v166", "v167", "v168", "v169", "v170", "v171", "v172", "v173", "v174", "v175", "v176", "v177", "v178", "v179", "v180", "v181", "v182", "v183", "v184", "v185", "v186", "v187", "v188", "v189", "v190", "v191", "v208", "v209", "v210", "v211", "v212", "v213", "v214", "v215", "v220", "v221", "v222", "v223", "v224", "v225", "v226", "v227", "v228", "v229", "v230", "v231", "v244", "v245", "v246", "v247"
#define ASM_R0A \
  "s_branch 9f\n\t" \
  "12:\n\t" \
  "s_add_u32 %[f], %[f], 1\n\t" \
  "s_cmp_lt_u32 %[f], 0x200000\n\t" \
  "s_cbranch_scc1 10f\n\t" \
  "s_branch 11f\n\t" \
  "22:\n\t" \
  "s_sub_u32 %[b], %[b], 1\n\t" \
  "s_cbranch_scc0 20f\n\t" \
  "s_mov_b32 %[b], 0\n\t" \
  "s_branch 21f\n\t" \
  "9:\n\t" \
  "v_mov_b32 v224, %[T]\n\t" \
  R_LEAD \
  "10:\n\t" \
  "buffer_load_dwordx4 v[132:135], v66, %[d], %[sv0] offen sc1\n\t" \
  "buffer_load_dwordx4 v[136:139], v66, %[d], %[sv1] offen sc1\n\t" \
  "13:\n\t" \
  "buffer_load_dwordx4 v[128:131], v65, %[d], %[sp0] offen sc1\n\t" \
  "buffer_load_dwordx4 v[140:143], v64, %[d], %[sp0] offen sc1\n\t" \
  "buffer_load_dwordx4 v[144:147], v64, %[d], %[sp0] offen offset:2048 sc1\n\t" \
  "buffer_load_dwordx4 v[148:151], v64, %[d], %[sp1] offen sc1\n\t" \
  "buffer_load_dwordx4 v[152:155], v64, %[d], %[sp1] offen offset:2048 sc1\n\t" \
  "s_waitcnt vmcnt(6)\n\t" \
  "v_max3_u32 v226, v132, v133, v134\n\t" \
  "v_max_u32 v226, v226, v135\n\t" \
  "s_waitcnt vmcnt(5)\n\t" \
  "v_max3_u32 v226, v226, v136, v137\n\t" \
  "v_max3_u32 v226, v226, v138, v139\n\t" \
  "s_waitcnt vmcnt(4)\n\t" \
  "v_mfma_f32_16x16x32_bf16 v[160:163], v[132:135], v[128:131], 0\n\t" \
  "v_mfma_f32_16x16x32_bf16 v[164:167], v[136:139], v[128:131], 0\n\t" \
  "v_max3_u32 v226, v226, v128, v129\n\t" \
  "v_max3_u32 v226, v226, v130, v131\n\t" \
  "s_waitcnt vmcnt(3)\n\t" \
  "v_mfma_f32_16x16x32_bf16 v[160:163], v[80:83], v[140:143], v[160:163]\n\t" \
  "v_mfma_f32_16x16x32_bf16 v[164:167], v[84:87], v[140:143], v[164:167]\n\t" \
  "v_max3_u32 v226, v226, v140, v141\n\t" \
  "v_max3_u32 v226, v226, v142, v143\n\t" \
  "s_waitcnt vmcnt(2)\n\t" \
  "v_mfma_f32_16x16x32_bf16 v[160:163], v[88:91], v[144:147], v[160:163]\n\t" \
  "v_mfma_f32_16x16x32_bf16 v[164:167], v[92:95], v[144:147], v[164:167]\n\t" \
  "v_max3_u32 v226, v226, v144, v145\n\t" \
  "v_max3_u32 v226, v226, v146, v147\n\t" \
  "s_waitcnt vmcnt(1)\n\t" \
  "v_mfma_f32_16x16x32_bf16 v[160:163], v[96:99], v[148:151], v[160:163]\n\t" \
  "v_mfma_f32_16x16x32_bf16 v[164:167], v[100:103], v[148:151], v[164:167]\n\t" \
  "v_max3_u32 v226, v226, v148, v149\n\t" \
  "v_max3_u32 v226, v226, v150, v151\n\t" \
  "s_waitcnt vmcnt(0)\n\t" \
  "v_mfma_f32_16x16x32_bf16 v[160:163], v[104:107], v[152:155], v[160:163]\n\t" \
  "v_mfma_f32_16x16x32_bf16 v[164:167], v[108:111], v[152:155], v[164:167]\n\t" \
  "v_max3_u32 v226, v226, v152, v153\n\t" \
  "v_max3_u32 v226, v226, v154, v155\n\t" \
  "v_cmp_eq_u32 vcc, -1, v226\n\t" \
  "s_cbranch_vccnz 12b\n\t" \
  "11:\n\t" \
  "s_nop 3\n\t" \
  "v_add_f32_dpp v220, v164, v160 row_ror:8 row_mask:0xf bank_mask:0xf\n\t" \
  "v_add_f32_dpp v221, v165, v161 row_ror:8 row_mask:0xf bank_mask:0xf\n\t" \
  "v_add_f32_dpp v222, v166, v162 row_ror:8 row_mask:0xf bank_mask:0xf\n\t" \
  "v_add_f32_dpp v223, v167, v163 row_ror:8 row_mask:0xf bank_mask:0xf\n\t" \
  "20:\n\t" \
  "s_barrier\n\t" \
  "ds_read_b128 v[168:171], v67 offset:1024\n\t" \
  "ds_read_b128 v[172:175], v67 offset:2048\n\t" \
  "ds_read_b128 v[176:179], v67 offset:3072\n\t" \
  "21:\n\t" \
  "s_waitcnt lgkmcnt(2)\n\t" \
  "v_pk_add_f32 v[220:221], v[220:221], v[168:169]\n\t" \
  "v_pk_add_f32 v[222:223], v[222:223], v[170:171]\n\t" \
  "s_waitcnt lgkmcnt(1)\n\t" \
  "v_pk_add_f32 v[220:221], v[220:221], v[172:173]\n\t" \
  "v_pk_add_f32 v[222:223], v[222:223], v[174:175]\n\t" \
  "s_waitcnt lgkmcnt(0)\n\t" \
  "v_pk_add_f32 v[220:221], v[220:221], v[176:177]\n\t" \
  "v_pk_add_f32 v[222:223], v[222:223], v[178:179]\n\t" \
  "v_cvt_pk_bf16_f32 v220, v220, v221\n\t" \
  "v_cvt_pk_bf16_f32 v221, v222, v223\n\t" \
  "v_min_u32 v222, -2, v220\n\t" \
  "v_min_u32 v223, -2, v221\n\t" \
  "v_min_u32 v220, -2, v220\n\t" \
  "v_min_u32 v221, -2, v221\n\t" \
  "s_nop 0\n\t" \
  "v_permlane16_swap_b32 v220, v222\n\t" \
  "v_permlane16_swap_b32 v221, v223\n\t" \
  "buffer_store_dwordx4 v[220:223], v74, %[d], %[so] offen sc1\n\t" \
  ""
#define CLOB_R0A "v128", "v129", "v130", "v131", "v132", "v133", "v134", "v135", "v136", "v137", "v138", "v139", "v140", "v141", "v142", "v143", "v144", "v145", "v146", "v147", "v148", "v149", "v150", "v151", "v152", "v153", "v154", "v155", "v160", "v161", "v162", "v163", "v164", "v165", "v166", "v167", "v168", "v169", "v170", "v171", "v172", "v173", "v174", "v175", "v176", "v177", "v178", "v179", "v220", "v221", "v222", "v223", "v224", "v226"
#define ASM_R1A \
  "s_branch 9f\n\t" \
  "12:\n\t" \
  "s_add_u32 %[f], %[f], 1\n\t" \
  "s_cmp_lt_u32 %[f], 0x200000\n\t" \
  "s_cbranch_scc1 10f\n\t" \
  "s_branch 11f\n\t" \
  "9:\n\t" \
  "v_mov_b32 v224, %[T]\n\t" \
  R_LEAD \
  "10:\n\t" \
  "buffer_load_dwordx4 v[132:135], v66, %[d], %[sv0] offen sc1\n\t" \
  "buffer_load_dwordx4 v[136:139], v66, %[d], %[sv1] offen sc1\n\t" \
  "13:\n\t" \
  "buffer_load_dwordx4 v[128:131], v65, %[d], %[sp0] offen sc1\n\t" \
  "buffer_load_dwordx4 v[140:143], v64, %[d], %[sp0] offen sc1\n\t" \
  "buffer_load_dwordx4 v[144:147], v64, %[d], %[sp0] offen offset:2048 sc1\n\t" \
  "buffer_load_dwordx4 v[148:151], v64, %[d], %[sp1] offen sc1\n\t" \
  "buffer_load_dwordx4 v[152:155], v64, %[d], %[sp1] offen offset:2048 sc1\n\t" \
  "s_waitcnt vmcnt(6)\n\t" \
  "v_max3_u32 v226, v132, v133, v134\n\t" \
  "v_max_u32 v226, v226, v135\n\t" \
  "s_waitcnt vmcnt(5)\n\t" \
  "v_max3_u32 v226, v226, v136, v137\n\t" \
  "v_max3_u32 v226, v226, v138, v139\n\t" \
  "s_waitcnt vmcnt(4)\n\t" \
  "v_mfma_f32_16x16x32_bf16 v[160:163], v[132:135], v[128:131], 0\n\t" \
  "v_mfma_f32_16x16x32_bf16 v[164:167], v[136:139], v[128:131], 0\n\t" \
  "v_max3_u32 v226, v226, v128, v129\n\t" \
  "v_max3_u32 v226, v226, v130, v131\n\t" \
  "s_waitcnt vmcnt(3)\n\t" \
  "v_mfma_f32_16x16x32_bf16 v[160:163], v[80:83], v[140:143], v[160:163]\n\t" \
  "v_mfma_f32_16x16x32_bf16 v[164:167], v[84:87], v[140:143], v[164:167]\n\t" \
  "v_max3_u32 v226, v226, v140, v141\n\t" \
  "v_max3_u32 v226, v226, v142, v143\n\t" \
  "s_waitcnt vmcnt(2)\n\t" \
  "v_mfma_f32_16x16x32_bf16 v[160:163], v[88:91], v[144:147], v[160:163]\n\t" \
  "v_mfma_f32_16x16x32_bf16 v[164:167], v[92:95], v[144:147], v[164:167]\n\t" \
  "v_max3_u32 v226, v226, v144, v145\n\t" \
  "v_max3_u32 v226, v226, v146, v147\n\t" \
  "s_waitcnt vmcnt(1)\n\t" \
  "v_mfma_f32_16x16x32_bf16 v[160:163], v[96:99], v[148:151], v[160:163]\n\t" \
  "v_mfma_f32_16x16x32_bf16 v[164:167], v[100:103], v[148:151], v[164:167]\n\t" \
  "v_max3_u32 v226, v226, v148, v149\n\t" \
  "v_max3_u32 v226, v226, v150, v151\n\t" \
  "s_waitcnt vmcnt(0)\n\t" \
  "v_mfma_f32_16x16x32_bf16 v[160:163], v[104:107], v[152:155], v[160:163]\n\t" \
  "v_mfma_f32_16x16x32_bf16 v[164:167], v[108:111], v[152:155], v[164:167]\n\t" \
  "v_max3_u32 v226, v226, v152, v153\n\t" \
  "v_max3_u32 v226, v226, v154, v155\n\t" \
  "v_cmp_eq_u32 vcc, -1, v226\n\t" \
  "s_cbranch_vccnz 12b\n\t" \
  "11:\n\t" \
  "s_nop 3\n\t" \
  "v_add_f32_dpp v220, v164, v160 row_ror:8 row_mask:0xf bank_mask:0xf\n\t" \
  "v_add_f32_dpp v221, v165, v161 row_ror:8 row_mask:0xf bank_mask:0xf\n\t" \
  "v_add_f32_dpp v222, v166, v162 row_ror:8 row_mask:0xf bank_mask:0xf\n\t" \
  "v_add_f32_dpp v223, v167, v163 row_ror:8 row_mask:0xf bank_mask:0xf\n\t" \
  "ds_write_b128 v67, v[220:223]\n\t" \
  "s_waitcnt lgkmcnt(0)\n\t" \
  "s_barrier\n\t" \
  ""
#define CLOB_R1A "v128", "v129", "v130", "v131", "v132", "v133", "v134", "v135", "v136", "v137", "v138", "v139", "v140", "v141", "v142", "v143", "v144", "v145", "v146", "v147", "v148", "v149", "v150", "v151", "v152", "v153", "v154", "v155", "v160", "v161", "v162", "v163", "v164", "v165", "v166", "v167", "v220", "v221", "v222", "v223", "v224", "v226"
#define ASM_R0B \
  "s_branch 9f\n\t" \
  "12:\n\t" \
  "s_add_u32 %[f], %[f], 1\n\t" \
  "s_cmp_lt_u32 %[f], 0x200000\n\t" \
  "s_cbranch_scc1 10f\n\t" \
  "s_branch 11f\n\t" \
  "22:\n\t" \
  "s_sub_u32 %[b], %[b], 1\n\t" \
  "s_cbranch_scc0 20f\n\t" \
  "s_mov_b32 %[b], 0\n\t" \
  "s_branch 21f\n\t" \
  "9:\n\t" \
  "v_mov_b32 v224, %[T]\n\t" \
  R_LEAD \
  "10:\n\t" \
  "buffer_load_dwordx4 v[132:135], v66, %[d], %[sv0] offen sc1\n\t" \
  "buffer_load_dwordx4 v[136:139], v66, %[d], %[sv1] offen sc1\n\t" \
  "13:\n\t" \
  "buffer_load_dwordx4 v[128:131], v65, %[d], %[sp0] offen sc1\n\t" \
  "buffer_load_dwordx4 v[140:143], v64, %[d], %[sp0] offen sc1\n\t" \
  "buffer_load_dwordx4 v[144:147], v64, %[d], %[sp0] offen offset:2048 sc1\n\t" \
  "buffer_load_dwordx4 v[148:151], v64, %[d], %[sp1] offen sc1\n\t" \
  "buffer_load_dwordx4 v[152:155], v64, %[d], %[sp1] offen offset:2048 sc1\n\t" \
  "s_waitcnt vmcnt(6)\n\t" \
  "v_max3_u32 v226, v132, v133, v134\n\t" \
  "v_max_u32 v226, v226, v135\n\t" \
  "s_waitcnt vmcnt(5)\n\t" \
  "v_max3_u32 v226, v226, v136, v137\n\t" \
  "v_max3_u32 v226, v226, v138, v139\n\t" \
  "s_waitcnt vmcnt(4)\n\t" \
  "v_mfma_f32_16x16x32_bf16 v[160:163], v[132:135], v[128:131], 0\n\t" \
  "v_mfma_f32_16x16x32_bf16 v[164:167], v[136:139], v[128:131], 0\n\t" \
  "v_max3_u32 v226, v226, v128, v129\n\t" \
  "v_max3_u32 v226, v226, v130, v131\n\t" \
  "s_waitcnt vmcnt(3)\n\t" \
  "v_mfma_f32_16x16x32_bf16 v[160:163], v[184:187], v[140:143], v[160:163]\n\t" \
  "v_mfma_f32_16x16x32_bf16 v[164:167], v[188:191], v[140:143], v[164:167]\n\t" \
  "v_max3_u32 v226, v226, v140, v141\n\t" \
  "v_max3_u32 v226, v226, v142, v143\n\t" \
  "s_waitcnt vmcnt(2)\n\t" \
  "v_mfma_f32_16x16x32_bf16 v[160:163], v[192:195], v[144:147], v[160:163]\n\t" \
  "v_mfma_f32_16x16x32_bf16 v[164:167], v[196:199], v[144:147], v[164:167]\n\t" \
  "v_max3_u32 v226, v226, v144, v145\n\t" \
  "v_max3_u32 v226, v226, v146, v147\n\t" \
  "s_waitcnt vmcnt(1)\n\t" \
  "v_mfma_f32_16x16x32_bf16 v[160:163], v[200:203], v[148:151], v[160:163]\n\t" \
  "v_mfma_f32_16x16x32_bf16 v[164:167], v[204:207], v[148:151], v[164:167]\n\t" \
  "v_max3_u32 v226, v226, v148, v149\n\t" \
  "v_max3_u32 v226, v226, v150, v151\n\t" \
  "s_waitcnt vmcnt(0)\n\t" \
  "v_mfma_f32_16x16x32_bf16 v[160:163], v[208:211], v[152:155], v[160:163]\n\t" \
  "v_mfma_f32_16x16x32_bf16 v[164:167], v[212:215], v[152:155], v[164:167]\n\t" \
  "v_max3_u32 v226, v226, v152, v153\n\t" \
  "v_max3_u32 v226, v226, v154, v155\n\t" \
  "v_cmp_eq_u32 vcc, -1, v226\n\t" \
  "s_cbranch_vccnz 12b\n\t" \
  "11:\n\t" \
  "s_nop 3\n\t" \
  "v_add_f32_dpp v220, v164, v160 row_ror:8 row_mask:0xf bank_mask:0xf\n\t" \
  "v_add_f32_dpp v221, v165, v161 row_ror:8 row_mask:0xf bank_mask:0xf\n\t" \
  "v_add_f32_dpp v222, v166, v162 row_ror:8 row_mask:0xf bank_mask:0xf\n\t" \
  "v_add_f32_dpp v223, v167, v163 row_ror:8 row_mask:0xf bank_mask:0xf\n\t" \
  "20:\n\t" \
  "s_barrier\n\t" \
  "ds_read_b128 v[168:171], v67 offset:1024\n\t" \
  "ds_read_b128 v[172:175], v67 offset:2048\n\t" \
  "ds_read_b128 v[176:179], v67 offset:3072\n\t" \
  "21:\n\t" \
  "s_waitcnt lgkmcnt(2)\n\t" \
  "v_pk_add_f32 v[220:221], v[220:221], v[168:169]\n\t" \
  "v_pk_add_f32 v[222:223], v[222:223], v[170:171]\n\t" \
  "s_waitcnt lgkmcnt(1)\n\t" \
  "v_pk_add_f32 v[220:221], v[220:221], v[172:173]\n\t" \
  "v_pk_add_f32 v[222:223], v[222:223], v[174:175]\n\t" \
  "s_waitcnt lgkmcnt(0)\n\t" \
  "v_pk_add_f32 v[220:221], v[220:221], v[176:177]\n\t" \
  "v_pk_add_f32 v[222:223], v[222:223], v[178:179]\n\t" \
  "v_cvt_pk_bf16_f32 v220, v220, v221\n\t" \
  "v_cvt_pk_bf16_f32 v221, v222, v223\n\t" \
  "v_min_u32 v222, -2, v220\n\t" \
  "v_min_u32 v223, -2, v221\n\t" \
  "v_min_u32 v220, -2, v220\n\t" \
  "v_min_u32 v221, -2, v221\n\t" \
  "s_nop 0\n\t" \
  "v_permlane16_swap_b32 v220, v222\n\t" \
  "v_permlane16_swap_b32 v221, v223\n\t" \
  "buffer_store_dwordx4 v[220:223], v74, %[d], %[so] offen sc1\n\t" \
  ""
#define CLOB_R0B "v128", "v129", "v130", "v131", "v132", "v133", "v134", "v135", "v136", "v137", "v138", "v139", "v140", "v141", "v142", "v143", "v144", "v145", "v146", "v147", "v148", "v149", "v150", "v151", "v152", "v153", "v154", "v155", "v160", "v161", "v162", "v163", "v164", "v165", "v166", "v167", "v168", "v169", "v170", "v171", "v172", "v173", "v174", "v175", "v176", "v177", "v178", "v179", "v220", "v221", "v222", "v223", "v224", "v226"
#define ASM_R1B \
  "s_branch 9f\n\t" \
  "12:\n\t" \
  "s_add_u32 %[f], %[f], 1\n\t" \
  "s_cmp_lt_u32 %[f], 0x200000\n\t" \
  "s_cbranch_scc1 10f\n\t" \
  "s_branch 11f\n\t" \
  "9:\n\t" \
  "v_mov_b32 v224, %[T]\n\t" \
  R_LEAD \
  "10:\n\t" \
  "buffer_load_dwordx4 v[132:135], v66, %[d], %[sv0] offen sc1\n\t" \
  "buffer_load_dwordx4 v[136:139], v66, %[d], %[sv1] offen sc1\n\t" \
  "13:\n\t" \
  "buffer_load_dwordx4 v[128:131], v65, %[d], %[sp0] offen sc1\n\t" \
  "buffer_load_dwordx4 v[140:143], v64, %[d], %[sp0] offen sc1\n\t" \
  "buffer_load_dwordx4 v[144:147], v64, %[d], %[sp0] offen offset:2048 sc1\n\t" \
  "buffer_load_dwordx4 v[148:151], v64, %[d], %[sp1] offen sc1\n\t" \
  "buffer_load_dwordx4 v[152:155], v64, %[d], %[sp1] offen offset:2048 sc1\n\t" \
  "s_waitcnt vmcnt(6)\n\t" \
  "v_max3_u32 v226, v132, v133, v134\n\t" \
  "v_max_u32 v226, v226, v135\n\t" \
  "s_waitcnt vmcnt(5)\n\t" \
  "v_max3_u32 v226, v226, v136, v137\n\t" \
  "v_max3_u32 v226, v226, v138, v139\n\t" \
  "s_waitcnt vmcnt(4)\n\t" \
  "v_mfma_f32_16x16x32_bf16 v[160:163], v[132:135], v[128:131], 0\n\t" \
  "v_mfma_f32_16x16x32_bf16 v[164:167], v[136:139], v[128:131], 0\n\t" \
  "v_max3_u32 v226, v226, v128, v129\n\t" \
  "v_max3_u32 v226, v226, v130, v131\n\t" \
  "s_waitcnt vmcnt(3)\n\t" \
  "v_mfma_f32_16x16x32_bf16 v[160:163], v[184:187], v[140:143], v[160:163]\n\t" \
  "v_mfma_f32_16x16x32_bf16 v[164:167], v[188:191], v[140:143], v[164:167]\n\t" \
  "v_max3_u32 v226, v226, v140, v141\n\t" \
  "v_max3_u32 v226, v226, v142, v143\n\t" \
  "s_waitcnt vmcnt(2)\n\t" \
  "v_mfma_f32_16x16x32_bf16 v[160:163], v[192:195], v[144:147], v[160:163]\n\t" \
  "v_mfma_f32_16x16x32_bf16 v[164:167], v[196:199], v[144:147], v[164:167]\n\t" \
  "v_max3_u32 v226, v226, v144, v145\n\t" \
  "v_max3_u32 v226, v226, v146, v147\n\t" \
  "s_waitcnt vmcnt(1)\n\t" \
  "v_mfma_f32_16x16x32_bf16 v[160:163], v[200:203], v[148:151], v[160:163]\n\t" \
  "v_mfma_f32_16x16x32_bf16 v[164:167], v[204:207], v[148:151], v[164:167]\n\t" \
  "v_max3_u32 v226, v226, v148, v149\n\t" \
  "v_max3_u32 v226, v226, v150, v151\n\t" \
  "s_waitcnt vmcnt(0)\n\t" \
  "v_mfma_f32_16x16x32_bf16 v[160:163], v[208:211], v[152:155], v[160:163]\n\t" \
  "v_mfma_f32_16x16x32_bf16 v[164:167], v[212:215], v[152:155], v[164:167]\n\t" \
  "v_max3_u32 v226, v226, v152, v153\n\t" \
  "v_max3_u32 v226, v226, v154, v155\n\t" \
  "v_cmp_eq_u32 vcc, -1, v226\n\t" \
  "s_cbranch_vccnz 12b\n\t" \
  "11:\n\t" \
  "s_nop 3\n\t" \
  "v_add_f32_dpp v220, v164, v160 row_ror:8 row_mask:0xf bank_mask:0xf\n\t" \
  "v_add_f32_dpp v221, v165, v161 row_ror:8 row_mask:0xf bank_mask:0xf\n\t" \
  "v_add_f32_dpp v222, v166, v162 row_ror:8 row_mask:0xf bank_mask:0xf\n\t" \
  "v_add_f32_dpp v223, v167, v163 row_ror:8 row_mask:0xf bank_mask:0xf\n\t" \
  "ds_write_b128 v67, v[220:223]\n\t" \
  "s_waitcnt lgkmcnt(0)\n\t" \
  "s_barrier\n\t" \
  ""
#define CLOB_R1B "v128", "v129", "v130", "v131", "v132", "v133", "v134", "v135", "v136", "v137", "v138", "v139", "v140", "v141", "v142", "v143", "v144", "v145", "v146", "v147", "v148", "v149", "v150", "v151", "v152", "v153", "v154", "v155", "v160", "v161", "v162", "v163", "v164", "v165", "v166", "v167", "v220", "v221", "v222", "v223", "v224", "v226"
#define W_IN(W) "{a[0:31]}"(W[0]), "{a[32:63]}"(W[1]), "{a[64:95]}"(W[2]), "{a[96:127]}"(W[3]), "{a[128:159]}"(W[4]), "{a[160:191]}"(W[5]), "{a[192:223]}"(W[6]), "{a[224:255]}"(W[7])

struct HeadCtx {
  LAS char* L;
  unsigned lbase;
  int lane, wave;
  unsigned qb[4];
};
DI bf16x16 frag4(LAS char* L, unsigned a0, unsigned a1, unsigned a2, unsigned a3) {
  s16x4 t0 = tr_read_l(L + a0), t1 = tr_read_l(L + a1), t2 = tr_read_l(L + a2), t3 = tr_read_l(L + a3);
  s16x8 a = __builtin_shufflevector(t0, t1, 0, 1, 2, 3, 4, 5, 6, 7);
  s16x8 b = __builtin_shufflevector(t2, t3, 0, 1, 2, 3, 4, 5, 6, 7);
  s16x16 c = __builtin_shufflevector(a, b, 0, 1, 2, 3, 4, 5, 6, 7, 8, 9, 10, 11, 12, 13, 14, 15);
  return __builtin_bit_cast(bf16x16, c);
}
DI void stage_half(const HeadCtx& c, const u16* W, int half) {
#pragma unroll 4
  for (int i = 0; i < 16; ++i) {
    int blk = half * 64 + c.wave * 16 + i;
    int row = blk * 4 + (c.lane >> 4);
    int ch = (c.lane & 15) ^ (((row & 3) << 2) | ((row >> 2) & 3));
    glds(W + (size_t)row * HD + ch * 8, c.lbase + L_ST + blk * 1024);
  }
}
DI bf16x16 proj_frag(const HeadCtx& c, int c2, int t) {
  unsigned add = ((t & 4) | (c.wave ^ (t & 3))) * 16384;
  asm volatile("" : "+s"(add));
  return frag4(c.L, (c.qb[0] ^ (c2 * 32)) + add, (c.qb[1] ^ (c2 * 32)) + add, (c.qb[2] ^ (c2 * 32)) + add, (c.qb[3] ^ (c2 * 32)) + add);
}
DI void head_ctx(HeadCtx& c, char* lds, int wave) {
  c.L = (LAS char*)lds;
  c.lbase = __builtin_amdgcn_readfirstlane((unsigned)(size_t)c.L);
  c.lane = lane_id(); c.wave = wave;
  const int gB = c.lane >> 4, q = (c.lane & 15) >> 2, pp = c.lane & 3;
#pragma unroll
  for (int u = 0; u < 4; ++u) {
    const int k = (u < 2) ? (8 * gB + 4 * u + q) : (32 + 8 * gB + 4 * (u - 2) + q);
    const int rq = 16 * (k >> 4) + 8 * ((k >> 1) & 1) + 2 * ((k >> 2) & 3) + (k & 1);
    c.qb[u] = L_ST + off_b(rq, 4 * wave + (pp >> 1)) + 8 * (pp & 1);
  }
}
template <int HF>
DI void frag_proj(LAS char* L, int lane, int wave, bf16x8 (&wf)[2][16]) {
  const int g = lane >> 4, r16 = lane & 15, q = r16 >> 2, pp = lane & 3;
#pragma unroll
  for (int t = 0; t < 2; ++t) {
    const unsigned o0 = off_b(8 * g + q, 4 * wave + pp) + 8 * t, o1 = off_b(8 * g + 4 + q, 4 * wave + pp) + 8 * t;
#pragma unroll
    for (int kk = 0; kk < 8; ++kk) wf[t][HF * 8 + kk] = tr_frag(L + o0 + kk * 8192, L + o1 + kk * 8192);
  }
}
DI void stage_row(const Params& p, unsigned ldst, int lane, int wave, int sg, const int i) {
  const unsigned vo = (unsigned)((lane ^ ((wave & 1) * 8)) * 16) ^ (unsigned)(i * 16 + (i >> 2) * 4096);
  unsigned keep;
  asm volatile("s_mov_b32 %0, m0\n\ts_mov_b32 m0, %3\n\ts_nop 0\n\tglobal_load_lds_dwordx4 %1, %2 offset:%4 sc1\n\ts_mov_b32 m0, %0"
               : "=&s"(keep) : "v"(vo), "s"(p.ws + WS_XN + (size_t)(unsigned)((sg * 4 + wave) * 8192)), "s"(ldst + wave * 8192 + (i >> 2) * 4096), "n"((i & 3) * 1024) : "memory");
}
#define HS_PEEK(v) asm volatile("buffer_load_dwordx2 %0, %1, %2, %3 offen sc1" : "=&v"(v) : "v"((unsigned)(lane & 31) * 2048), "s"(mkdesc(p.ex + EX_FLAGS, F_END)), "s"(H_A + hs.blk * 8) : "memory")
DI void a_head(const Params& p, const Hs& hs, int h, char* lds, const int wave) {
  HeadCtx c; head_ctx(c, lds, wave);
  const int lane = c.lane, g = lane >> 4, r16 = lane & 15;
  LAS char* L = c.L;
  const rsrc_t rex = mkrsrc(p.ex, EX_FLAGS);
  const rsrc_t rc = mkrsrc(p.ex + EX_FLAGS, F_END);
  const int sidx = r16 < 8 ? 0x4444 : 0xEEEE;
  unsigned budget = SPIN_BUDGET;
  Rate rb = {0u, 0u};
  u32x32 W[8];
#define LOADW(i, c2, hf, reg) do { W[i] = cat4(proj_frag(c, c2, hf * 4), proj_frag(c, c2, hf * 4 + 1), proj_frag(c, c2, hf * 4 + 2), proj_frag(c, c2, hf * 4 + 3)); asm volatile("" : "+{" reg "}"(W[i])); } while (0)
  u32x4 kq0[2][2];
  bf16x8 wf[2][16];
  f32x4 ac[2][2];
  bf16x8 xf[8][2];
  auto rows = [&](int hf) {
#pragma unroll
    for (int kk = 0; kk < 8; ++kk)
#pragma unroll
      for (int m = 0; m < 2; ++m) xf[kk][m] = *(const LAS bf16x8*)(L + (m * 16 + r16) * 1024 + (((4 * (hf * 8 + kk) + g) ^ r16) * 16));
    SCHED();
  };
  auto prod = [&](int hf, int k0, int k1, bool fresh) {
#pragma unroll
    for (int kk = k0; kk < k1; ++kk)
#pragma unroll
      for (int m = 0; m < 2; ++m)
#pragma unroll
        for (int t = 0; t < 2; ++t) { if (fresh && kk == 0) MFV0(ac[m][t], wf[t][hf * 8], xf[kk][m]); else MFV(ac[m][t], wf[t][hf * 8 + kk], xf[kk][m]); }
  };
  auto keep = [&](int mat) {
    asm volatile("s_nop 7\n\ts_nop 1" : "+v"(ac[0][0]), "+v"(ac[0][1]), "+v"(ac[1][0]), "+v"(ac[1][1]));
#pragma unroll
    for (int m = 0; m < 2; ++m) kq0[mat][m] = u32x4{pack2(ac[m][0][0], ac[m][0][1]), pack2(ac[m][0][2], ac[m][0][3]), pack2(ac[m][1][0], ac[m][1][1]), pack2(ac[m][1][2], ac[m][1][3])};
  };
  auto fetch_rows = [&]() {
#pragma unroll
    for (int i = 0; i < 8; ++i) stage_row(p, c.lbase, lane, wave, 0, i);
  };
  stage_half(c, p.wq + h * 128, 0); stage_half(c, p.wq + h * 128, 1);
  WAITV(16); barrier_raw();
  LOADW(0, 0, 0, "a[0:31]"); LOADW(2, 1, 0, "a[64:95]");
  frag_proj<0>(L + L_ST, lane, wave, wf);
  barrier_raw();
  u32x2 hv, hw;
  HS_PEEK(hv);
  stage_half(c, p.wk + h * 128, 0);
  WAITV(16); barrier_raw();
  PIN_V(hv);
  const bool early = hs_eq(hv, hs);
  if (early) fetch_rows();
  HS_PEEK(hw);
  LOADW(1, 0, 1, "a[32:63]"); LOADW(3, 1, 1, "a[96:127]");
  frag_proj<1>(L + L_ST + 65536, lane, wave, wf);
  barrier_raw();
  stage_half(c, p.wk + h * 128, 1);
  WAITV(16);
  PIN_V(hw);
  if (!early) { if (!hs_eq(hw, hs)) hs_spin(rc, hs, budget); fetch_rows(); WAITV(0); }
  if (wave == 0) hs_post(rc, hs, ~0u);
  unsigned pv = __builtin_amdgcn_raw_buffer_load_b32(rc, 0, F_PAR, 16);
  barrier_raw();
  rows(1); prod(1, 0, 4, true); LOADW(4, 0, 0, "a[128:159]"); prod(1, 4, 8, false);
  rows(0); prod(0, 0, 4, false); LOADW(6, 1, 0, "a[192:223]"); prod(0, 4, 8, false);
  frag_proj<0>(L + L_ST, lane, wave, wf);
  keep(1);
  prod(0, 0, 8, true);
  rows(1);
  WAITV(0); barrier_raw();
  frag_proj<1>(L + L_ST + 65536, lane, wave, wf);
  prod(1, 0, 4, false); LOADW(5, 0, 1, "a[160:191]"); prod(1, 4, 8, false); LOADW(7, 1, 1, "a[224:255]");
  keep(0);
#pragma unroll
  for (int i = 0; i < 4; ++i) *(LAS u32x4*)(L + L_ST + (i * 4 + wave) * 1024 + lane * 16) = kq0[i >> 1][i & 1];
  pv = __builtin_amdgcn_readfirstlane(pv);
  if (pv == 0xffffffffu) pv = wait_par(rc, budget);
  const unsigned par = pv & 1u, cpy = ((pv >> 1) ^ h) & 1u;
  const rsrc_t rpt = mkrsrc(p.ws + WS_PT, 4 * MB);
  const float sc = 0.08838834764831845f * 1.4426950408889634f;
  const unsigned lane16 = lane * 16;
  u32x4 xa;
#pragma unroll
  for (int j = 0; j < 4; ++j) xa[j] = c.lbase + L_XB + ((lane16 + wave * 1024) ^ (j * 1024));
  const unsigned qkw = c.lbase + L_QK + (wave * 32 + (lane >> 5) * 16 + r16) * 32 + (g & 1) * 8;
  const u32x4 dex = mkdesc(p.ex, EX_FLAGS);
  unsigned sb = SPIN_BUDGET, nf = 0;
  LAS char* qk_r = L + L_QK + (g * 8 + (r16 >> 2)) * 32 + (lane & 1) * 8;
  LAS char* flw = L + L_FL + wave * 256 + lane * 4;
  LAS char* flr = L + L_FL + (lane & 3) * 256 + lane * 4;
  barrier_raw();
  { const unsigned T = 0; FL_SET(0); FL_SET(1024); FL_SET(2048); }
  volatile LAS int* kqw = (volatile LAS int*)(L + L_FL + 3072 + lane * 4);
  *kqw = 0;
  volatile LAS unsigned* fbw = (volatile LAS unsigned*)(L + L_FL + 3328);
  *fbw = 0;
  barrier_raw();
  auto run = [&](auto role_c) {
    constexpr int ROLE = decltype(role_c)::v;
    bf16x8 k1[4], k2[4], qf[4];
    f32x4 s1, s2;
    const float NEG = -3.0e38f;
    auto tok_scores = [&](int bb) {
#pragma unroll
      for (int kk = 0; kk < 4; ++kk) {
        LAS char* kq = L + L_ST + bb * 16384 + lane16 + kk * 1024;
        k1[kk] = *(const LAS bf16x8*)kq; k2[kk] = *(const LAS bf16x8*)(kq + 4096); qf[kk] = *(const LAS bf16x8*)(kq + 8192 + (wave - 1) * 4096);
      }
      MFV0(s1, k1[0], qf[0]); MFV0(s2, k2[0], qf[0]);
#pragma unroll
      for (int kk = 1; kk < 4; ++kk) { MFV(s1, k1[kk], qf[kk]); MFV(s2, k2[kk], qf[kk]); }
      asm volatile("s_nop 7\n\ts_nop 1" : "+v"(s1), "+v"(s2));
      const int lim1 = wave == 1 ? r16 : 64, lim2 = wave == 1 ? -1 : r16;
#pragma unroll
      for (int r = 0; r < 4; ++r) {
        if (g * 4 + r > lim1) s1[r] = NEG;
        if (g * 4 + r > lim2) s2[r] = NEG;
      }
      asm volatile("" : "+v"(s1), "+v"(s2));
    };
    const unsigned vo_pt = ROLE == 0 ? (unsigned)(g * 128 + (r16 & 7) * 16 + (r16 >> 3) * 8) : 0u;
    const unsigned vo_pm = ROLE == 0 ? (g < 2 ? (unsigned)(512 + (r16 & 7) * 16 + g * 8 + (r16 >> 3) * 4) : OOB)
                                     : (g < 2 ? (unsigned)(1536 + (wave - 1) * 256 + r16 * 16 + g * 8) : OOB);
    const unsigned long long him = 0xff00ff00ff00ff00ull;
    unsigned t0 = 0, rec = 0xffffffffu;
    int ready = 0, fetched = 0;
    int b3 = 0;
    unsigned rq = 0, fk = 0xffffffffu;
    if constexpr (ROLE == 1) tok_scores(0);
    for (int s = 0; s < NSEG; ++s) {
      const unsigned T = s + 1;
      const unsigned o0 = exphys(EX_MEM + (s & 3) * 16384 + cpy * 8192, par) + wave * 1024;
      if constexpr (ROLE == 2) {
        if (s > 0 && rec != 0xffffffffu) rec -= ready < s + 2 ? PKE : 0;
      }
      if (s > 0) sched_wait(rec, t0, ROLE == 0 ? DA0 : DA1);
      if constexpr (ROLE == 2) {
        if (ready < s + 2) {
          asm volatile("buffer_load_dword %0, %1, %2, %3 offen sc1" : "+v"(fk) : "v"((unsigned)(min(ready + 1 + (lane >> 2), NSEG - 1) * 4 + (lane & 3)) * 4), "s"(mkdesc(p.ex + EX_FLAGS, F_END)), "s"(F_QK + h * 1024) : "memory");
          if (s > 0 && rec != 0xffffffffu) sched_wait(rec + PKE, t0, 0);
        }
      }
      const unsigned nf0 = nf;
#define KFALL() do { FENCE(); _Pragma("unroll") for (int kk = 0; kk < 4; ++kk) kf[kk] = tr_frag(qk_r + kk * 1024 + 16, qk_r + kk * 1024 + 144); FENCE(); } while (0)
      if constexpr (ROLE == 0) {
        const unsigned sp = exphys(EX_P + (unsigned)(s & 3) * 32768 + h * 1024, par);
        asm volatile(ASM_A0
                     : [b] "+s"(sb), [f] "+s"(nf)
                     : W_IN(W), "{v[112:115]}"(xa), [xo] "s"((unsigned)(s & 1) * 8192), "{v64}"(lane16), "{v69}"(qkw), "{v70}"(laddr(qk_r)), "{v71}"(laddr(flw)), "{v72}"(laddr(flr)), "{v73}"(sidx),
                       "{v74}"(vo_pt), "{v75}"(vo_pm), [kb] "s"(c.lbase + L_ST + b3 * 16384),
                       [d] "s"(dex), [o0] "s"(o0), [o1] "s"(o0 + 4096), [T] "s"(T), [hi] "s"(him), [sp] "s"(sp), [sc] "s"(sc), [nsc] "s"(-sc)
                     : CLOB_A0, "vcc", "scc", "memory");
      } else {
        asm volatile(ASM_A1
                     : [b] "+s"(sb), [f] "+s"(nf)
                     : W_IN(W), "{v[112:115]}"(xa), [xo] "s"((unsigned)(s & 1) * 8192), "{v64}"(lane16), "{v69}"(qkw), "{v71}"(laddr(flw)), "{v72}"(laddr(flr)), "{v73}"(sidx),
                       [d] "s"(dex), [o0] "s"(o0), [o1] "s"(o0 + 4096), [T] "s"(T)
                     : CLOB_A1, "vcc", "scc", "memory");
      }
      t0 = (unsigned)__builtin_amdgcn_s_memtime();
      if constexpr (ROLE != 0) { if (nf != nf0) *fbw = T; }
      if constexpr (ROLE == 2) __builtin_amdgcn_s_sleep(QUIET_A);
      unsigned rs;
      if constexpr (ROLE == 2) REC_ASK(rs, (unsigned)(s + 1) * 128);
      else rec = __builtin_amdgcn_raw_buffer_load_b32(rc, 0, F_REC + (unsigned)(s + 1) * 128, 16);
      if constexpr (ROLE == 0) {
        if ((s & RMK) == 0) rq = (rate(rb) | (s ? 0u : 0x7fffu)) << 16;
        st32(rq | (unsigned)(s << 1) | (nf != nf0 || *fbw == T ? 1u : 0u), rex, lane == 0 ? 640u : OOB, exphys(EX_P + (unsigned)(s & 3) * 32768 + h * 1024, par));
      } else if constexpr (ROLE == 1) {
        bf16x8 kf[4];
        f32x4 sm;
        if (s > 0 && s <= NSEG - NARM) set_flag(rc, FPT(s - 1, h, wave - 1));
        for (;;) {
          unsigned flk = FL_GET(2048);
          KFALL();
          if (__all(flk == T) || budget == 0) break;
          --budget;
        }
        MFV0(sm, kf[0], qf[0]); MFV(sm, kf[1], qf[1]); MFV(sm, kf[2], qf[2]); MFV(sm, kf[3], qf[3]);
        float mx = vmax3(vmax3(s1[0], s1[1], s1[2]), vmax3(s1[3], s2[0], s2[1]), vmax(s2[2], s2[3]));
        asm volatile("s_nop 7\n\ts_nop 1" : "+v"(sm), "+v"(mx) :: "memory");
        mx = vmax3(vmax3(sm[0], sm[1], sm[2]), sm[3], mx);
        mx = xmax32(xmax16(mx));
        const float nm = -mx * sc;
        f32x4 a, b, cc;
#pragma unroll
        for (int r = 0; r < 4; ++r) {
          a[r] = __builtin_amdgcn_exp2f(s1[r] * sc + nm);
          b[r] = __builtin_amdgcn_exp2f(s2[r] * sc + nm);
          cc[r] = __builtin_amdgcn_exp2f(sm[r] * sc + nm);
        }
        float ls = ((a[0] + a[1]) + (a[2] + a[3])) + ((b[0] + b[1]) + (b[2] + b[3])) + (g < 2 ? ((cc[0] + cc[1]) + (cc[2] + cc[3])) : 0.f);
        ls = xsum32(xsum16(ls));
        const float inv = __builtin_amdgcn_rcpf(ls);
        u32x4 ptk = {pack2(a[0] * inv, a[1] * inv), pack2(a[2] * inv, a[3] * inv), pack2(b[0] * inv, b[1] * inv), pack2(b[2] * inv, b[3] * inv)};
        u32x2 pm = {pack2(cc[0] * inv, cc[1] * inv), pack2(cc[2] * inv, cc[3] * inv)};
        const unsigned so = (unsigned)(s * NH + h) * 2048;
        asm volatile("" : "+v"(ptk), "+v"(pm));
        if (s < NSEG - 1) __builtin_amdgcn_s_sleep(QUIET_T);
        asm volatile("" ::: "memory");
        if (wave == 1) { u32x2 lo = {ptk[0], ptk[1]}; st64(lo, rpt, lane * 8, so); }
        else st128(ptk, rpt, lane16 + 512, so);
        st64(pm, rpt, vo_pm, so);
        if (s >= NSEG - NARM) set_flag(rc, FPT(s, h, wave - 1));
      } else if constexpr (ROLE == 2) {
        *kqw = fetched;
        if (ready < s + 2) {
          PIN_V(fk);
          const unsigned long long miss = __ballot(fk == 0xffffffffu);
          ready += miss ? (int)(__builtin_ctzll(miss) >> 2) : 16;
        }
        if (AEARLY) {
        const u32x4 sent = {0xffffffffu, 0xffffffffu, 0xffffffffu, 0xffffffffu};
        st128(sent, rex, lane < 40 ? lane16 : OOB, exphys(EX_P + (unsigned)((s + 3) & 3) * 32768 + h * 1024, par));
        }
        while (fetched < min(s + 2, NSEG - 1)) {
          asm volatile("" ::: "memory");
          if (ready <= fetched) {
            const int sg = min(ready + 1 + (lane >> 2), NSEG - 1);
            const unsigned v = __builtin_amdgcn_raw_buffer_load_b32(rc, (unsigned)(sg * 4 + (lane & 3)) * 4, F_QK + h * 1024, 16);
            const unsigned long long miss = __ballot(v == 0xffffffffu);
            const int n = miss ? (int)(__builtin_ctzll(miss) >> 2) : 16;
            if (n > 0) ready += n;
            else if (fetched > s && s >= BLK) break;
            else if (budget == 0) ready = NSEG;
            else { --budget; __builtin_amdgcn_s_sleep(3); continue; }
          }
          asm volatile("" ::: "memory");
          ++fetched;
          const size_t sq = (size_t)(fetched * NH + h) * 8192 + lane16;
          const int bn = fetched == s + 1 ? (b3 == 2 ? 0 : b3 + 1) : (b3 == 0 ? 2 : b3 - 1);
          const unsigned dst = c.lbase + L_ST + bn * 16384;
#pragma unroll
          for (int pc = 0; pc < 16; ++pc) glds_c(p.ws + (pc < 8 ? WS_K : WS_Q) + sq + (pc & 7) * 1024, dst + pc * 1024);
          if (fetched == s + 1) { WAITV(0); *kqw = fetched; }
        }
        if (!AEARLY) {
        const u32x4 sent = {0xffffffffu, 0xffffffffu, 0xffffffffu, 0xffffffffu};
        st128(sent, rex, lane < 40 ? lane16 : OOB, exphys(EX_P + (unsigned)((s + 3) & 3) * 32768 + h * 1024, par));
        }
      }
      if constexpr (ROLE == 2) { REC_GET(rs); rec = rs; }
      b3 = b3 == 2 ? 0 : b3 + 1;
      if constexpr (ROLE < 2) {
        FENCE();
        while (s + 1 < NSEG && __any(*kqw <= s) && budget != 0) { --budget; __builtin_amdgcn_s_sleep(1); }
        FENCE();
      }
      if constexpr (ROLE == 1) tok_scores(b3);
    }
  };
  if (wave == 0) run(IC<0>{});
  else if (wave < 3) run(IC<1>{});
  else run(IC<2>{});
}

DI void v_head(const Params& p, const Hs& hs, int h, char* lds, const int wave) {
  HeadCtx c; head_ctx(c, lds, wave);
  const int lane = c.lane, g = lane >> 4, r16 = lane & 15;
  LAS char* L = c.L;
  const rsrc_t rex = mkrsrc(p.ex, EX_FLAGS);
  const rsrc_t rc = mkrsrc(p.ex + EX_FLAGS, F_END);
  const int sidx = r16 < 8 ? 0x4444 : 0xEEEE;
  unsigned budget = SPIN_BUDGET;
  u32x32 W[8];
  unsigned ob[4];
  {
    const int gB = lane >> 4, q = (lane & 15) >> 2, pp = lane & 3;
#pragma unroll
    for (int u = 0; u < 4; ++u) {
      const int k = (u < 2) ? (8 * gB + 4 * u + q) : (32 + 8 * gB + 4 * (u - 2) + q);
      const int ro = 8 * (k >> 4) + 2 * ((k >> 2) & 3) + (k & 1);
      ob[u] = L_ST + wave * 8192 + off_b(ro, 2 * ((k >> 1) & 1) + (pp >> 1)) + 8 * (pp & 1);
    }
  }
  auto stage_wo = [&](int j) {
#pragma unroll
    for (int i = 0; i < 8; ++i) {
      int row = i * 4 + (lane >> 4);
      int ch = (lane & 15) ^ (((row & 3) << 2) | ((row >> 2) & 3));
      glds(p.wo + ((size_t)h * 128 + (wave ^ j) * 32 + row) * DM + wave * 128 + ch * 8, c.lbase + L_ST + j * 32768 + wave * 8192 + i * 1024);
    }
  };
  auto wo_frag = [&](int cp, int j) {
    unsigned add = j * 32768;
    asm volatile("" : "+s"(add));
    return frag4(L, (ob[0] ^ (cp * 64)) + add, (ob[1] ^ (cp * 64)) + add, (ob[2] ^ (cp * 64)) + add, (ob[3] ^ (cp * 64)) + add);
  };
#define LOADO(j, reg) do { W[4 + j] = cat4(wo_frag(0, j), wo_frag(1, j), wo_frag(2, j), wo_frag(3, j)); asm volatile("" : "+{" reg "}"(W[4 + j])); } while (0)
  for (int i = 0; i < 64 && now32() - hs.lo < VDEL; ++i) __builtin_amdgcn_s_sleep(4);
  stage_half(c, p.wv + h * 128, 0); stage_half(c, p.wv + h * 128, 1);
  WAITV(16); barrier_raw();
  LOADW(0, 0, 0, "a[0:31]"); LOADW(2, 1, 0, "a[64:95]");
  barrier_raw();
  stage_wo(0); stage_wo(1);
  WAITV(16); barrier_raw();
  LOADW(1, 0, 1, "a[32:63]"); LOADW(3, 1, 1, "a[96:127]");
  barrier_raw();
  stage_wo(2); stage_wo(3);
  WAITV(16);
  LOADO(0, "a[128:159]"); LOADO(1, "a[160:191]");
  WAITV(0);
  LOADO(2, "a[192:223]"); LOADO(3, "a[224:255]");

  hs_wait(rc, hs, wave, budget);
  barrier_raw();
  const unsigned pv = wait_par(rc, budget), par = pv & 1u, cpy = ((pv >> 1) ^ h) & 1u;
  const unsigned lane16 = lane * 16;
  u32x4 xa;
#pragma unroll
  for (int j = 0; j < 4; ++j) xa[j] = c.lbase + L_XB + ((lane16 + wave * 1024) ^ (j * 1024));
  u32x4 vr;
#pragma unroll
  for (int j = 0; j < 4; ++j) vr[j] = c.lbase + L_QK + (((g * 8 + (r16 >> 2)) * 32 + (lane & 1) * 8 + wave * 1024) ^ (j * 1024));
  const unsigned qkw = c.lbase + L_QK + (wave * 32 + (lane >> 5) * 16 + r16) * 32 + (g & 1) * 8;
  const unsigned part_off = (unsigned)((wave * 128 + (g & 1) * 32 + (g >> 1) * 16 + r16) * 16);
  const u32x4 dex = mkdesc(p.ex, EX_FLAGS), dvo = mkdesc(p.ws + WS_VO, 16 * MB), dfl = mkdesc(p.ex + EX_FLAGS, F_END);
  unsigned sb = SPIN_BUDGET, nf = 0;
  LAS char* flw = L + L_FL + wave * 256 + lane * 4;
  LAS char* flr = L + L_FL + (lane & 3) * 256 + lane * 4;
  { const unsigned T = 0; FL_SET(0); FL_SET(1024); }
  barrier_raw();
  unsigned t0 = 0, rec = 0xffffffffu;

  for (int s = 0; s < NSEG; ++s) {
    const unsigned T = s + 1;
    const unsigned o0 = exphys(EX_MEM + (s & 3) * 16384 + cpy * 8192, par) + wave * 1024;
    const unsigned sr = s < NSEG - 1 ? exphys(EX_VO + (unsigned)((s & 1) * 32 + h) * 8192, par) : (unsigned)(s * NH + h) * 8192;
    const u32x4 ds = s < NSEG - 1 ? dex : dvo;
    if (s > 0) sched_wait(rec, t0, DV);
    const unsigned nf0 = nf;
    asm volatile(ASM_V
                 : [b] "+s"(sb), [f] "+s"(nf), "={v250}"(rec)
                 : W_IN(W), "{v[112:115]}"(xa), [xo] "s"((unsigned)(s & 1) * 8192), "{v64}"(lane16), "{v69}"(qkw), "{v71}"(laddr(flw)), "{v72}"(laddr(flr)), "{v73}"(sidx), "{v75}"(part_off), "{v[76:79]}"(vr),
                   [d] "s"(dex), [o0] "s"(o0), [o1] "s"(o0 + 4096), [T] "s"(T), [sr] "s"(sr), [ds] "s"(ds), [d2] "s"(dvo), [s2] "s"((unsigned)(s * NH + h) * 8192),
                   [d3] "s"(dfl), [s3] "s"(F_REC + (unsigned)(s + 1) * 128)
                 : CLOB_V, "vcc", "scc", "memory");
    sb = __builtin_amdgcn_readfirstlane(sb); nf = __builtin_amdgcn_readfirstlane(nf);
    t0 = (unsigned)__builtin_amdgcn_s_memtime();
    asm volatile("s_waitcnt vmcnt(2)" : "+{v250}"(rec) :: "memory");
    if (s > 0 && s <= NSEG - NARM) set_flag(rc, FVO(s - 1, wave, h));
  }
}

template <bool W0>
DI void reducer_t(const Params& p, int ct, char* lds, const int wave, u32x16& vb0, u32x16& vb1, unsigned& rec) {
  const int lane = lane_id(), g = lane >> 4, r16 = lane & 15;
  LAS char* L = (LAS char*)lds;
  const rsrc_t rex = mkrsrc(p.ex, EX_FLAGS);
  const rsrc_t rc = mkrsrc(p.ex + EX_FLAGS, F_END);
  unsigned budget = SPIN_BUDGET;
  const unsigned lane16 = lane * 16;
  const unsigned vo_p = (unsigned)((r16 >> 3) * 1024 + g * 128 + (r16 & 7) * 16);
  const unsigned vo_m = (unsigned)((4 * (r16 >> 3) + g) * 1024 + 512 + (r16 & 7) * 16);
  const unsigned vo_v = (unsigned)(g * 16384 + (ct * 16 + r16) * 16);
  const unsigned vo_o = r16 < 8 ? (unsigned)((g & 1) * 16384 + (2 * ct + (g >> 1)) * 128 + r16 * 16) : OOB;
  const unsigned par = wait_par(rc, budget) & 1u;
  LAS char* flw = L + L_FL + wave * 256 + lane * 4;
  LAS char* flr = L + L_FL + (1 + lane % 3) * 256 + lane * 4;
  { const unsigned T = 0; FL_SET(0); }
  barrier_raw();
  u32x16 va0, va1;
  const u32x4 dvw = mkdesc(p.ws + WS_VW, 64 * MB);
  const u32x4 dex = mkdesc(p.ex, EX_FLAGS);
  unsigned sb = SPIN_BUDGET, nf = 0;
  const unsigned lp = laddr(L) + wave * 1024 + lane16;
  const u32x4 sent = {0xffffffffu, 0xffffffffu, 0xffffffffu, 0xffffffffu};
  int ready = -1;
  auto need_tile = [&](int tile, bool block) {
    while (ready < tile) {
      asm volatile("" ::: "memory");
      const int tl = min(tile + (lane >> 3), NSEG - 1);
      unsigned v = __builtin_amdgcn_raw_buffer_load_b32(rc, FVW(tl, ct >> 3, wave * 8 + (lane & 7)), 0, 16);
      const unsigned long long miss = __ballot(v == 0xffffffffu);
      const int n = miss ? (int)(__builtin_ctzll(miss) >> 3) : 8;
      if (n > 0) ready = tile + n - 1;
      else if (!block) break;
      else if (budget == 0) ready = NSEG;
      else { --budget; __builtin_amdgcn_s_sleep(3); }
    }
    asm volatile("" ::: "memory");
  };
  unsigned fv = 0xffffffffu;
  int fbase = 0;
  const u32x4 dfl = mkdesc(p.ex + EX_FLAGS, F_END);
  auto peek = [&](int s) {
    if (ready < s + PEEKN) {
      fbase = ready + 1;
      asm volatile("buffer_load_dword %0, %1, %2, 0 offen sc1" : "+v"(fv) : "v"(FVW(min(fbase + (lane >> 3), NSEG - 1), ct >> 3, wave * 8 + (lane & 7))), "s"(dfl) : "memory");
    }
  };
  auto take = [&]() {
    PIN_V(fv);
    const unsigned long long miss = __ballot(fv == 0xffffffffu);
    ready = max(ready, fbase - 1 + (miss ? (int)(__builtin_ctzll(miss) >> 3) : 8));
  };
#define VWLD(r0, r1, r2, r3, r4, r5, r6, r7)                                                                                     \
  "buffer_load_dwordx4 " r0 ", %2, %3, %4 offen sc1\n\tbuffer_load_dwordx4 " r1 ", %2, %3, %4 offen offset:1024 sc1\n\t"          \
  "buffer_load_dwordx4 " r2 ", %2, %3, %4 offen offset:2048 sc1\n\tbuffer_load_dwordx4 " r3 ", %2, %3, %4 offen offset:3072 sc1\n\t" \
  "buffer_load_dwordx4 " r4 ", %2, %3, %5 offen sc1\n\tbuffer_load_dwordx4 " r5 ", %2, %3, %5 offen offset:1024 sc1\n\t"          \
  "buffer_load_dwordx4 " r6 ", %2, %3, %5 offen offset:2048 sc1\n\tbuffer_load_dwordx4 " r7 ", %2, %3, %5 offen offset:3072 sc1"
#define PREFETCH_A(s_) do { need_tile((s_), true); const unsigned so_ = (unsigned)(((s_) * 32 + ct) * 32 + wave * 8) * 1024;             \
    asm volatile(VWLD("v[80:83]", "v[84:87]", "v[88:91]", "v[92:95]", "v[96:99]", "v[100:103]", "v[104:107]", "v[108:111]")             \
                 : "=&{v[80:95]}"(va0), "=&{v[96:111]}"(va1) : "v"(lane16), "s"(dvw), "s"(so_), "s"(so_ + 4096) : "memory"); } while (0)
#define PREFETCH_B(s_) do { need_tile((s_), true); const unsigned so_ = (unsigned)(((s_) * 32 + ct) * 32 + wave * 8) * 1024;             \
    asm volatile(VWLD("v[184:187]", "v[188:191]", "v[192:195]", "v[196:199]", "v[200:203]", "v[204:207]", "v[208:211]", "v[212:215]")   \
                 : "=&{v[184:199]}"(vb0), "=&{v[200:215]}"(vb1) : "v"(lane16), "s"(dvw), "s"(so_), "s"(so_ + 4096) : "memory"); } while (0)
  unsigned t0 = 0, rs;
  rec = 0xffffffffu;
  unsigned tA = TA0 + XA, tB = TB0 + XB, pb_prev = 0, th2 = 0, tr1 = 0, tr2 = 0, good = 0;
  unsigned hst = 0x7fff0000u, sA = 0, sB = 0;
  Rate rb = {0u, 0u};
  rate(rb);
  bool pend = false;
  auto lead = [&](int s, bool ok, unsigned sp) {
    const unsigned pb = now32();
    PIN_V(hst);
    if (pend && s >= WFROM) { if (__any((hst & 0xffffu) == ((unsigned)((s - 1) << 1) | 1u))) tB += UPS; else tA += UPS; }
    if ((s & RMK) == 1 || s == 0) {
      sA = stretch(TA0 * SCA, QA0, min(__builtin_amdgcn_readlane(hst, 0), __builtin_amdgcn_readlane(hst, 1)) >> 16);
      sB = stretch(TB0 * SCB, QB0, rate(rb));
      if (s < 2) sA = sB + SA0;
    }
    if (s == NARROW) { tA -= XA; tB -= XB; }
    asm volatile("buffer_load_dword %0, %1, %2, %3 offen sc1" : "+v"(hst) : "v"((unsigned)(lane & 7) * 1024 + 640), "s"(dex), "s"(sp) : "memory");
    ok = ok && !__any(*(volatile LAS unsigned*)(flr + 1024) == ((unsigned)(s << 1) | 1u));
    const bool on = tr1 != 0 && ok;
    pend = tr1 != 0 && good >= GOODN && !ok;
    good = on ? good + 1 : 0;
    unsigned th;
    if (tr2 != 0 && (on || (int)(th2 - pb) >= REB)) th = tr2 + tB + sB;
    else th = pb + (s == 0 ? (unsigned)E0 : min(max(pb - pb_prev, (unsigned)EMIN), (unsigned)EMAX) + ESL);
    th2 = th;
    const unsigned tr = th + tA + sA;
    if (s >= PUBF) { u32x2 r = {nosent(th), nosent(tr)}; st64(r, rc, lane == 0 ? 0u : OOB, F_REC + (unsigned)(s + 2) * 128); }
    tr1 = tr2; tr2 = s >= PUBF ? max(tr, 1u) : 0u; pb_prev = pb;
  };
#define R_IN "{v64}"(vo_p), "{v65}"(vo_m), "{v66}"(vo_v), "{v67}"(lp), "{v71}"(laddr(flw)), "{v72}"(laddr(flr)), "{v74}"(vo_o), \
             [d] "s"(dex), [sv0] "s"(sv), [sv1] "s"(sv + 65536), [sp0] "s"(sp), [sp1] "s"(sp + 4096), [T] "s"(T), [so] "s"(so)
#define R_STEP(s, B, X, VIN)                                                                                                     \
  {                                                                                                                              \
    const unsigned T = (s) + 1;                                                                                                  \
    const unsigned sp = exphys(EX_P + (unsigned)((s) & 3) * 32768 + wave * 8192, par);                                           \
    const unsigned sv = exphys(EX_VO + (unsigned)(B * 32 + wave * 8) * 8192, par);                                               \
    const unsigned so = exphys(EX_MEM + (unsigned)(((s) + 1) & 3) * 16384, par);                                                  \
    if ((s) > 0) sched_wait(rec, t0, DR);                                                                                        \
    const unsigned nf0 = nf;                                                                                                     \
    if (W0) {                                                                                                                    \
      asm volatile(ASM_R0##X : [b] "+s"(sb), [f] "+s"(nf) : R_IN, VIN : CLOB_R0##X, "vcc", "scc", "memory");                            \
      if (ct == 0) lead(s, nf == nf0, sp);                                                                                       \
      __builtin_amdgcn_s_sleep(QUIET_R0);                                                                                        \
                                                       \
      st128(sent, rex, vo_o, exphys(EX_MEM + (unsigned)((s) & 3) * 16384, par));                                                  \
    } else {                                                                                                                     \
      asm volatile(ASM_R1##X : [b] "+s"(sb), [f] "+s"(nf) : R_IN, VIN : CLOB_R1##X, "vcc", "scc", "memory");               \
      *(volatile LAS unsigned*)(flw + 1024) = (unsigned)((s) << 1) | (nf != nf0 ? 1u : 0u);                                       \
      __builtin_amdgcn_s_sleep(QUIET_R);                            \
    }                                                                                                                            \
    t0 = (unsigned)__builtin_amdgcn_s_memtime();                                                                                 \
    REC_ASK(rs, (unsigned)((s) + 1) * 128 + 4);                                    \
    if (!RLATE) { st128(sent, rex, vo_v, sv); st128(sent, rex, vo_v, sv + 65536); }                                  \
  }
#define VIN_A "{v[80:95]}"(va0), "{v[96:111]}"(va1)
#define VIN_B "{v[184:199]}"(vb0), "{v[200:215]}"(vb1)
  PREFETCH_A(0);
  bool lateB = ready < 1;
  asm volatile("" : "={v[184:199]}"(vb0), "={v[200:215]}"(vb1));
  if (!lateB) PREFETCH_B(1);
  for (int s = 0; s < NSEG - 2; s += 2) {
    R_STEP(s, 0, A, VIN_A);
    take();
    if (s == 0 && ready < 2) need_tile(ready + 1, false);
    if (lateB) PREFETCH_B(s + 1);
    const bool lateA = ready < s + 2;
    if (!lateA) PREFETCH_A(s + 2);
    if (RLATE) { const unsigned sv = exphys(EX_VO + (unsigned)(wave * 8) * 8192, par); st128(sent, rex, vo_v, sv); st128(sent, rex, vo_v, sv + 65536); }
    peek(s);
    REC_GET(rs); rec = rs;
    R_STEP(s + 1, 1, B, VIN_B);
    take();
    if (lateA) PREFETCH_A(s + 2);
    lateB = ready < s + 3;
    if (!lateB) PREFETCH_B(s + 3);
    if (RLATE) { const unsigned sv = exphys(EX_VO + (unsigned)(32 + wave * 8) * 8192, par); st128(sent, rex, vo_v, sv); st128(sent, rex, vo_v, sv + 65536); }
    peek(s + 1);
    REC_GET(rs); rec = rs;
  }
  R_STEP(NSEG - 2, 0, A, VIN_A);
  if (lateB) PREFETCH_B(NSEG - 1);
  REC_GET(rs); rec = rs;
  asm volatile("s_waitcnt vmcnt(0)" : "+{v[184:199]}"(vb0), "+{v[200:215]}"(vb1) : "v"(fv), "v"(hst) : "memory");
}
DI bf16x8 frag_of(const u32x16& v, int j) { u32x4 r = {v[4 * j], v[4 * j + 1], v[4 * j + 2], v[4 * j + 3]}; return __builtin_bit_cast(bf16x8, r); }
DI void reducer(int ct, char* lds, int wave) {
 for (int it = 0; it < 2; ++it) {
  u32x16 vb[2] = {};
  unsigned rec = 0;
  asm volatile("" : "+s"(ct), "+s"(wave));
  if (it == (ct ? 1 : 0)) {
    unsigned budget = SPIN_BUDGET;
    volatile LAS unsigned* nw = (volatile LAS unsigned*)((LAS char*)lds + L_TOTAL - 8);
    const Hs hs = {(unsigned)__builtin_amdgcn_readfirstlane(nw[0]), (unsigned)__builtin_amdgcn_readfirstlane(nw[1]), (unsigned)(64 + (ct & 1) * 32 + (ct >> 1))};
    if (ct == 0) calibrate(args_again(), hs, lds, wave, budget); else ln_rest(args_again(), hs, ct, wave, budget);
  }
  asm volatile("" : "+s"(ct), "+s"(wave));
  if (it) { if (wave == 0) reducer_t<true>(args_again(), ct, lds, wave, vb[0], vb[1], rec); else reducer_t<false>(args_again(), ct, lds, wave, vb[0], vb[1], rec); }
  asm volatile("" : "+s"(ct), "+s"(wave));
  const Params p = args_again();
  unsigned off = it ? 0u : OOB;
  constexpr int s = NSEG - 1;
  const int lane = lane_id(), g = lane >> 4, r16 = lane & 15;
  LAS char* L = (LAS char*)lds + 32768;
  const rsrc_t rc = mkrsrc(p.ex + EX_FLAGS, F_END);
  const rsrc_t rvo = mkrsrc(p.ws + WS_VO, 16 * MB);
  const rsrc_t rpt = mkrsrc(p.ws + WS_PT, 4 * MB);
  rec = __builtin_amdgcn_readfirstlane(rec);
  if (rec == 0xffffffffu) rec = now32() + 100;
  st32(nosent(rec + TO + TH), rc, lane == 0 && wave == 0 && ct < 8 ? off : OOB, F_H + s * 4 + (ct & 7) * 256);
  const u32x4 xr = *(const u32x4*)(p.x + (size_t)(s * 32 + (lane >> 1)) * DM + ct * 16 + (lane & 1) * 8);
  unsigned budget = SPIN_BUDGET;
  f32x4 acc[2];
  sched_wait(rec + TO, 0, 0);
  for (;;) {
    asm volatile("" : "+v"(off) :: "memory");
    u32x2 p0[8];
    bf16x8 p1[8], pm[2][2], vo[2];
    const unsigned sp = (unsigned)(s * NH + wave * 8) * 2048;
#pragma unroll
    for (int i = 0; i < 2; ++i) vo[i] = ldfrag(rvo, off | (unsigned)(g * 8192 + (ct * 16 + r16) * 16), (unsigned)(s * NH + wave * 8 + i * 4) * 8192);
#pragma unroll
    for (int j = 0; j < 8; ++j) { p1[j] = ldfrag(rpt, off | (unsigned)(lane * 16 + 512), sp + j * 2048); p0[j] = ld64(rpt, off | (unsigned)(lane * 8), sp + j * 2048); }
#pragma unroll
    for (int i = 0; i < 2; ++i)
#pragma unroll
      for (int qt = 0; qt < 2; ++qt) pm[i][qt] = ldfrag(rpt, off | (unsigned)(g * 2048 + 1536 + r16 * 16 + qt * 256), sp + i * 8192);
    SCHED();
    unsigned m = max(umax4f(vo[0]), umax4f(vo[1]));
    acc[0] = f32x4{0.f, 0.f, 0.f, 0.f}; acc[1] = acc[0];
#pragma unroll
    for (int j = 0; j < 8; ++j) {
      u32x4 z0 = {p0[j][0], p0[j][1], 0u, 0u};
      acc[1] = MF(frag_of(vb[j >> 2], j & 3), p1[j], acc[1]);
      acc[0] = MF(frag_of(vb[j >> 2], j & 3), __builtin_bit_cast(bf16x8, z0), acc[0]);
      m = max(max(m, umax4f(p1[j])), max(p0[j][0], p0[j][1]));
    }
#pragma unroll
    for (int i = 0; i < 2; ++i) {
      acc[0] = MF(vo[i], pm[i][0], acc[0]); acc[1] = MF(vo[i], pm[i][1], acc[1]);
      m = max(m, max(umax4f(pm[i][0]), umax4f(pm[i][1])));
    }
    if (__all(m != 0xffffffffu) || budget == 0) break;
    --budget;
  }
#pragma unroll
  for (int qt = 0; qt < 2; ++qt) *(LAS f32x4*)(L + wave * 2560 + (qt * 16 + r16) * 80 + g * 16) = acc[qt];
  barrier_raw();
  if (wave == 0) {
    u32x4 o;
#pragma unroll
    for (int ps = 0; ps < 2; ++ps) {
      f32x4 hv = {__uint_as_float(xr[2 * ps] << 16), __uint_as_float(xr[2 * ps] & 0xffff0000u), __uint_as_float(xr[2 * ps + 1] << 16), __uint_as_float(xr[2 * ps + 1] & 0xffff0000u)};
      LAS char* a = L + (lane >> 1) * 80 + (lane & 1) * 32 + ps * 16;
      f32x4 u0 = *(const LAS f32x4*)(a), u1 = *(const LAS f32x4*)(a + 2560), u2 = *(const LAS f32x4*)(a + 2 * 2560), u3 = *(const LAS f32x4*)(a + 3 * 2560);
      hv += (u0 + u1) + (u2 + u3);
      o[2 * ps] = pack2(hv[0], hv[1]); o[2 * ps + 1] = pack2(hv[2], hv[3]);
    }
    st128(nosent4(o), mkrsrc(p.ws + WS_H, 4 * MB), off | (unsigned)(((lane >> 1) * DM + ct * 16 + (lane & 1) * 8) * 2), (unsigned)s * (32 * DM * 2));
  }
 }
}

DI void stage_proj(const u16* W, unsigned lbase, int lane, int wave, int hf, unsigned dst) {
#pragma unroll 4
  for (int i = 0; i < 16; ++i) {
    int blk = wave * 16 + i;
    int row = blk * 4 + (lane >> 4);
    int ch = (lane & 15) ^ (((row & 3) << 2) | ((row >> 2) & 3));
    glds(W + (size_t)(hf * 256 + row) * HD + ch * 8, lbase + dst + blk * 1024);
  }
}
static_assert(NH2 <= 8 && NH2 + 2 <= NARM, "armed rows of h");
#define XNB(sg) (131072 - ((sg) & 1) * 114688)
DI void stage_xn1(const Params& p, unsigned lbase, int lane, int wave, int sg, int buf, const int i) { stage_row(p, lbase + XNB(buf), lane, wave, sg, i); }
DI void stage_xn(const Params& p, unsigned lbase, int lane, int wave, int sg, int buf) {
#pragma unroll
  for (int i = 0; i < 8; ++i) stage_xn1(p, lbase, lane, wave, sg, buf, i);
}
#define NMID 8

DI void qk_worker(const Params& p, const Hs& hs, int h, char* lds, const int wave) {
  const int lane = lane_id(), g = lane >> 4, r16 = lane & 15;
  LAS char* L = (LAS char*)lds;
  const unsigned lbase = __builtin_amdgcn_readfirstlane((unsigned)(size_t)L);
  const rsrc_t rc = mkrsrc(p.ex + EX_FLAGS, F_END);
  unsigned budget = SPIN_BUDGET;
  bf16x8 wf[2][2][16];
  stage_proj(p.wk + h * 128, lbase, lane, wave, 0, 0);
  stage_proj(p.wk + h * 128, lbase, lane, wave, 1, 65536);
  WAITV(16); barrier_raw();
  frag_proj<0>(L, lane, wave, wf[1]);
  barrier_raw();
  stage_proj(p.wq + h * 128, lbase, lane, wave, 0, 0);
  WAITV(16); barrier_raw();
  frag_proj<1>(L + 65536, lane, wave, wf[1]);
  barrier_raw();
  u32x2 hv, hw;
  HS_PEEK(hv);
  stage_proj(p.wq + h * 128, lbase, lane, wave, 1, 65536);
  HS_PEEK(hw);
  WAITV(17);
  PIN_V(hv);
  const bool early = hs_eq(hv, hs);
  if (early) stage_xn(p, lbase, lane, wave, 1, 0);
  barrier_raw();
  frag_proj<0>(L, lane, wave, wf[0]);
  if (early) WAITV(8); else WAITV(0);
  PIN_V(hw);
  if (!early) { if (!hs_eq(hw, hs)) hs_spin(rc, hs, budget); stage_xn(p, lbase, lane, wave, 1, 0); }
  if (wave == 0) hs_post(rc, hs, ~0u);
  barrier_raw();
#define QB(sg) ((((sg) + 2) & 3) * 32768 + 32768)
#pragma unroll
  for (int i = 0; i < 8; ++i) stage_row(p, lbase + QB(2), lane, wave, 2, i);
  frag_proj<1>(L + 65536, lane, wave, wf[0]);
  WAITV(8);
  barrier_raw();
  const rsrc_t ro = mkrsrc(p.ws + WS_Q, 32 * MB);
  const unsigned fq = F_QK + (unsigned)h * 1024 + wave * 4;
  f32x4 acc[2][2][2] = {};
  bf16x8 xq[QPD];
  auto xrd = [&](LAS char* A, int t) { return *(const LAS bf16x8*)(A + (t >> 4) * 16384 + (((4 * (t & 15) + g) ^ r16) * 16)); };
  auto put = [&](int sl, int m, int sg, unsigned vo) {
    const f32x4 &a = acc[sl][m][0], &b = acc[sl][m][1];
    u32x4 v = {pack2(a[0], a[1]), pack2(a[2], a[3]), pack2(b[0], b[1]), pack2(b[2], b[3])};
    st128(v, ro, vo, (unsigned)(sl * 16 * MB) + (unsigned)(sg * NH + h) * 8192 + (m * 4 + wave) * 1024);
  };
#pragma unroll
  for (int t = 0; t < QPD; ++t) xq[t] = xrd(L + QB(1) + r16 * 1024, t);
  for (int sg = 1; sg < NSEG; ++sg) {
    if (sg == 6) wait_flags(rc, (lane & 31) * 128, F_XN, budget);
    if (sg >= 16 && sg < 16 + 4 * NARM) {
      const u32x4 sent = {0xffffffffu, 0xffffffffu, 0xffffffffu, 0xffffffffu};
      unsigned hw4 = h * 4 + wave;
      char* wsb = p.ws;
      asm volatile("" : "+s"(hw4), "+s"(wsb));
      const rsrc_t rw = mkrsrc(wsb + WS_VO, (unsigned)(WS_ACT + 6 * MB - WS_VO));
      const unsigned t = NSEG - NARM - 4 + (sg >> 2), pc = sg & 3;
      unsigned so = t * (NH * 8192) + hw4 * 2048 + pc * 1024, lim = 64;
      if (pc == 2) { so = (unsigned)(WS_PT - WS_VO) + t * (NH * 2048) + hw4 * 512; lim = 32; }
      if (pc == 3) {
        if (t < NSEG - NH2) so = (unsigned)(WS_H - WS_VO) + (NSEG - 4 - (t & 1) * 4) * 32768 + hw4 * 1024;
        else { so = (unsigned)(WS_ACT - WS_VO) + t * 90112 + hw4 * 1024; lim = hw4 < 88 ? 64 : 0; }
      }
      st128(sent, rw, (unsigned)lane < lim ? lane * 16 : OOB, so);
    }
    const int sn = min(sg + 2, NSEG - 1);
    LAS char* A = L + QB(sg) + r16 * 1024;
    LAS char* An = L + QB(sg + 1) + r16 * 1024;
    const unsigned vp = sg > 1 ? lane * 16 : OOB;
    bf16x8 xf[32];
#pragma unroll
    for (int t = 0; t < QPD; ++t) xf[t] = xq[t];
    SCHED();
#pragma unroll
    for (int t = 0; t < 32; ++t) {
      const int m = t >> 4, kk = t & 15;
      if (t + QPD < 32) xf[t + QPD] = xrd(A, t + QPD); else xq[t + QPD - 32] = xrd(An, t + QPD - 32);
#pragma unroll
      for (int sl = 0; sl < 2; ++sl)
#pragma unroll
        for (int c = 0; c < 2; ++c) acc[sl][m][c] = MF(wf[sl][c][kk], xf[t], kk ? acc[sl][m][c] : f32x4{});
      if (t == 1 || t == 2) put(2 - t, 1, sg - 1, vp);
      if (t >= 3 && t < 11) stage_row(p, lbase + QB(sg + 2), lane, wave, sn, t - 3);
      if (t == QMID && sg <= NMID) { WAITV(8); set_flag(rc, fq + (sg - 1) * 16); }
      if (t == 17 || t == 18) put(18 - t, 0, sg, lane * 16);
      if (t == 31 - QPD) {
        WAITV(10);
        set_flag(rc, fq + (sg - 1) * 16);
        asm volatile("" ::: "memory");
        __builtin_amdgcn_s_barrier();
        asm volatile("" ::: "memory");
      }
      SCHED();
    }
  }
  put(1, 1, NSEG - 1, lane * 16); put(0, 1, NSEG - 1, lane * 16);
  WAITV(0);
  set_flag(rc, fq + (NSEG - 1) * 16);
  barrier_raw();
}

DI void vw_worker(const Params& p, const Hs& hs, int h, int sw, char* lds, const int wave) {
  const int lane = lane_id(), g = lane >> 4, r16 = lane & 15;
  LAS char* L = (LAS char*)lds;
  const unsigned lbase = __builtin_amdgcn_readfirstlane((unsigned)(size_t)L);
  const rsrc_t rc = mkrsrc(p.ex + EX_FLAGS, F_END);
  unsigned budget = SPIN_BUDGET;
  bf16x8 wf[2][16], wo[8][4];
  const int so = SWAPO ? sw : 0;
  const int xl = lane ^ (so * 32);
  auto seg = [&](int i) { return min(i < KH ? 2 * i + sw : i + KH, NSEG - 1); };
  auto stage_wo = [&](int hf) {
#pragma unroll 4
    for (int i = 0; i < 16; ++i) {
      int row = i * 4 + (lane >> 4);
      int ch = (lane & 15) ^ (((row & 3) << 2) | ((row >> 2) & 3));
      glds(p.wo + ((size_t)h * 128 + (hf ^ so) * 64 + row) * DM + wave * 128 + ch * 8, lbase + hf * 65536 + wave * 16384 + i * 1024);
    }
  };
#define FRAG_WO(hf)                                                                                          \
  {                                                                                                          \
    const int q = r16 >> 2, pp = lane & 3;                                                                   \
    _Pragma("unroll") for (int n = 0; n < 8; ++n) {                                                          \
      const unsigned o0 = hf * 65536 + wave * 16384 + off_b(8 * g + q, 2 * n + (pp >> 1)) + 8 * (pp & 1);                 \
      const unsigned o1 = hf * 65536 + wave * 16384 + off_b(8 * g + 4 + q, 2 * n + (pp >> 1)) + 8 * (pp & 1);             \
      _Pragma("unroll") for (int kk = 0; kk < 2; ++kk) wo[n][hf * 2 + kk] = tr_frag(L + o0 + kk * 8192, L + o1 + kk * 8192); \
    }                                                                                                        \
  }
  stage_proj(p.wv + h * 128, lbase, lane, wave, so, 0);
  stage_proj(p.wv + h * 128, lbase, lane, wave, so ^ 1, 65536);
  WAITV(16); barrier_raw();
  frag_proj<0>(L, lane, wave, wf);
  barrier_raw();
  u32x2 hv, hw;
  HS_PEEK(hv);
  stage_wo(0);
  WAITV(16); barrier_raw();
  PIN_V(hv);
  const bool early = hs_eq(hv, hs);
  if (early) stage_xn(p, lbase, xl, wave, seg(0), 0);
  HS_PEEK(hw);
  frag_proj<1>(L + 65536, lane, wave, wf);
  barrier_raw();
  stage_wo(1);
  WAITV(16);
  PIN_V(hw);
  if (!early) { if (!hs_eq(hw, hs)) hs_spin(rc, hs, budget); stage_xn(p, lbase, xl, wave, seg(0), 0); WAITV(0); }
  if (wave == 0) hs_post(rc, hs, ~0u);
  FRAG_WO(0);
  const rsrc_t ro = mkrsrc(p.ws + WS_VW, 64 * MB);
  LAS char* VX = L + lane * 16;
  bf16x8 xf[16][2];
  auto x_first = [&](int i) {
    LAS char* A = L + XNB(i) + r16 * 1024;
#pragma unroll
    for (int kk = 0; kk < 6; ++kk)
#pragma unroll
      for (int m = 0; m < 2; ++m) xf[kk][m] = *(const LAS bf16x8*)(A + m * 16384 + (((4 * kk + g) ^ r16) * 16));
  };
  auto v_part = [&](int i) {
    LAS char* A = L + XNB(i) + r16 * 1024;
    f32x4 acc[2][2] = {};
#pragma unroll
    for (int kk = 0; kk < 16; ++kk) {
      if (kk + 6 < 16) {
#pragma unroll
        for (int m = 0; m < 2; ++m) xf[kk + 6][m] = *(const LAS bf16x8*)(A + m * 16384 + (((4 * (kk + 6) + g) ^ r16) * 16));
      }
#pragma unroll
      for (int m = 0; m < 2; ++m) { acc[m][0] = MF(wf[0][kk], xf[kk][m], acc[m][0]); acc[m][1] = MF(wf[1][kk], xf[kk][m], acc[m][1]); }
      if (kk == VK && i == 1) { WAITV(0); set_flag(rc, FVW(sw, wave, h)); }
      SCHED();
    }
#pragma unroll
    for (int m = 0; m < 2; ++m) {
      const f32x4 &a = acc[m][0], &b = acc[m][1];
      u32x4 v = {pack2(a[0], a[1]), pack2(a[2], a[3]), pack2(b[0], b[1]), pack2(b[2], b[3])};
      *(LAS u32x4*)(VX + (i & 1) * 8192 + (m * 4 + (wave ^ (so * 2))) * 1024) = v;
    }
  };
  barrier_raw();
  x_first(0);
  stage_xn(p, lbase, xl, wave, seg(1), 1);
  SCHED();
  v_part(0);
  WAITV(8);
  FRAG_WO(1);
  WAITV(0);
  const int nu = sw ? KH : NSEG - KH;
  int sp = 0;
  for (int i = 0; i < nu; ++i) {
    if (i == (KH >= 4 ? 2 : 6 - KH)) wait_flags(rc, (lane & 31) * 128, F_XN, budget);
    barrier_raw();
    const int sg = seg(i), sn = seg(i + 2);
    bf16x8 vf[2][4];
#pragma unroll
    for (int kk = 0; kk < 4; ++kk)
#pragma unroll
      for (int m = 0; m < 2; ++m) vf[m][kk] = *(const LAS bf16x8*)(VX + (i & 1) * 8192 + (m * 4 + kk) * 1024);
    x_first(i + 1);
    SCHED();
    f32x4 a2[8][2] = {};
#pragma unroll
    for (int n = 0; n <= 8; ++n) {
      if (n < 8) {
#pragma unroll
        for (int kk = 0; kk < 4; ++kk)
#pragma unroll
          for (int m = 0; m < 2; ++m) a2[n][m] = MF(vf[m][kk], wo[n][kk], a2[n][m]);
      }
      if (n > 0) {
        const f32x4 &a = a2[n - 1][0], &b = a2[n - 1][1];
        u32x4 v = {pack2(a[0], a[1]), pack2(a[2], a[3]), pack2(b[0], b[1]), pack2(b[2], b[3])};
        st128(v, ro, lane * 16, (unsigned)((sg * 32 + wave * 8 + n - 1) * 32 + h) * 1024);
      }
      __builtin_amdgcn_sched_group_barrier(0x008, 2, 0);
      __builtin_amdgcn_sched_group_barrier(0x002, 4, 0);
      __builtin_amdgcn_sched_group_barrier(0x008, 2, 0);
      __builtin_amdgcn_sched_group_barrier(0x002, 4, 0);
      __builtin_amdgcn_sched_group_barrier(0x008, 2, 0);
      __builtin_amdgcn_sched_group_barrier(0x002, 4, 0);
      __builtin_amdgcn_sched_group_barrier(0x008, 2, 0);
      __builtin_amdgcn_sched_group_barrier(0x040, 1, 0);
      SCHED();
      if (n < 4) { stage_xn1(p, lbase, xl, wave, sn, i, 2 * n); stage_xn1(p, lbase, xl, wave, sn, i, 2 * n + 1); SCHED(); }
    }
    v_part(i + 1);
    if (i < VMID) { WAITV(0); sp = sg; }
    else WAITV(5);
    set_flag(rc, FVW(sp, wave, h));
    sp = sg;
  }
  WAITV(0);
  set_flag(rc, FVW(sp, wave, h));
  barrier_raw();
}

template <class F>
DI void starter(const Params& p, const Hs& hs, int tile, const int wave, F&& meanwhile) {
  const int lane = lane_id();
  const rsrc_t rc = mkrsrc(p.ex + EX_FLAGS, F_END);
  const u32x4 sent = {0xffffffffu, 0xffffffffu, 0xffffffffu, 0xffffffffu};
  for (unsigned i = tile * 4 + wave; i < F_IEND / 1024; i += 128) st128(sent, rc, lane * 16, i * 1024);
  ln_rows<1>(p, tile * 8 + wave * 2);
  const u32x2 l0 = ld64(rc, (unsigned)(wave * 64 + lane) * 128, H_R);
  WAITV(0);
  barrier_raw();
  hs_serve(rc, hs, tile, wave, l0, meanwhile);
}

DI void out_worker(const Params& p, int cb, int ph, char* lds, const int wave) {
  const int lane = lane_id(), g = lane >> 4, r16 = lane & 15;
  LAS char* L = (LAS char*)lds;
  const rsrc_t rc = mkrsrc(p.ex + EX_FLAGS, F_END);
  unsigned budget = SPIN_BUDGET;
  const rsrc_t rvw = mkrsrc(p.ws + WS_VW, 64 * MB);
  const rsrc_t rvo = mkrsrc(p.ws + WS_VO, 16 * MB);
  const rsrc_t rpt = mkrsrc(p.ws + WS_PT, 4 * MB);
  const rsrc_t rh = mkrsrc(p.ws + WS_H, 4 * MB);
  const unsigned lane16 = lane * 16;
  const unsigned vo_pm = (unsigned)(g * 2048 + 1536 + r16 * 16);
  const unsigned vo_vo = (unsigned)(g * 8192 + (cb * 64 + r16) * 16);
  const int orow = wave * 8 + (lane >> 3), oc = (lane & 7) * 8;
  for (int s = ph; s < NSEG - 1; s += 4) {
    bf16x8 vw[8][4];
    wait_flags(rc, FVW(s, cb >> 1, wave * 8 + (lane & 7)), 0, budget);
#pragma unroll
    for (int t = 0; t < 4; ++t)
#pragma unroll
      for (int j = 0; j < 4; ++j) {
        const unsigned so = (unsigned)((s * 32 + cb * 4 + t) * 32 + wave * 8) * 1024;
        vw[j][t] = ldfrag(rvw, lane16 + j * 1024, so);
        vw[4 + j][t] = ldfrag(rvw, lane16 + j * 1024, so + 4096);
      }
    const u32x4 xr = *(const u32x4*)(p.x + (size_t)(s * 32 + orow) * DM + cb * 64 + oc);
    {
      const unsigned fo = lane < 16 || s >= NSEG - NARM ? FPT(s, wave * 8 + ((lane >> 1) & 7), lane & 1) : FVO(s, cb >> 1, wave * 8 + (lane & 7));
      wait_flags_lazy(rc, fo, 0, budget, s < NSEG - NLAZY);
    }
    f32x4 acc[4][2];
    for (;;) {
      asm volatile("" ::: "memory");
      u32x2 p0[8];
      bf16x8 p1[8], pm[2][2], vo[2][4];
      const unsigned sp = (unsigned)(s * NH + wave * 8) * 2048;
#pragma unroll
      for (int j = 0; j < 8; ++j) { p1[j] = ldfrag(rpt, lane16 + 512, sp + j * 2048); p0[j] = ld64(rpt, lane * 8, sp + j * 2048); }
#pragma unroll
      for (int i = 0; i < 2; ++i)
#pragma unroll
        for (int qt = 0; qt < 2; ++qt) pm[i][qt] = ldfrag(rpt, vo_pm + qt * 256, sp + i * 8192);
#pragma unroll
      for (int i = 0; i < 2; ++i)
#pragma unroll
        for (int t = 0; t < 4; ++t) vo[i][t] = ldfrag(rvo, vo_vo + t * 256, (unsigned)(s * NH + wave * 8 + i * 4) * 8192);
      SCHED();
      unsigned m = 0;
#pragma unroll
      for (int t = 0; t < 4; ++t) { acc[t][0] = f32x4{0.f, 0.f, 0.f, 0.f}; acc[t][1] = acc[t][0]; }
#pragma unroll
      for (int j = 0; j < 8; ++j) {
        u32x4 z0 = {p0[j][0], p0[j][1], 0u, 0u};
        const bf16x8 b0 = __builtin_bit_cast(bf16x8, z0);
#pragma unroll
        for (int t = 0; t < 4; ++t) { acc[t][1] = MF(vw[j][t], p1[j], acc[t][1]); acc[t][0] = MF(vw[j][t], b0, acc[t][0]); }
        m = max(max(m, umax4f(p1[j])), max(p0[j][0], p0[j][1]));
        if (j == OHJ) { SCHED(); announce(rc, wave == 0 && s >= NSEG - NH2 && __all(m != 0xffffffffu), F_H + (unsigned)s * 4 + cb * 256, OD); SCHED(); }
      }
#pragma unroll
      for (int i = 0; i < 2; ++i) {
        m = max(m, max(umax4f(pm[i][0]), umax4f(pm[i][1])));
#pragma unroll
        for (int t = 0; t < 4; ++t) { acc[t][0] = MF(vo[i][t], pm[i][0], acc[t][0]); acc[t][1] = MF(vo[i][t], pm[i][1], acc[t][1]); m = max(m, umax4f(vo[i][t])); }
      }
      if (s < NSEG - NARM || __all(m != 0xffffffffu) || budget == 0) break;
      --budget;
    }
#pragma unroll
    for (int t = 0; t < 4; ++t)
#pragma unroll
      for (int qt = 0; qt < 2; ++qt) *(LAS f32x4*)(L + wave * 8704 + (qt * 16 + r16) * 272 + (t * 16 + g * 4) * 4) = acc[t][qt];
    barrier_raw();
    {
      u32x4 o;
#pragma unroll
      for (int ps = 0; ps < 2; ++ps) {
        f32x4 hv = {__uint_as_float(xr[2 * ps] << 16), __uint_as_float(xr[2 * ps] & 0xffff0000u), __uint_as_float(xr[2 * ps + 1] << 16), __uint_as_float(xr[2 * ps + 1] & 0xffff0000u)};
        LAS char* a = L + orow * 272 + oc * 4 + ps * 16;
        f32x4 u0 = *(const LAS f32x4*)(a), u1 = *(const LAS f32x4*)(a + 8704), u2 = *(const LAS f32x4*)(a + 2 * 8704), u3 = *(const LAS f32x4*)(a + 3 * 8704);
        hv += (u0 + u1) + (u2 + u3);
        o[2 * ps] = pack2(hv[0], hv[1]); o[2 * ps + 1] = pack2(hv[2], hv[3]);
      }
      st128(nosent4(o), rh, (unsigned)((orow * DM + cb * 64 + oc) * 2), (unsigned)s * (32 * DM * 2));
    }
    WAITV(0);
    barrier_raw();
    if (wave == 0) set_flag(rc, F_H + (unsigned)s * 4 + cb * 256);
  }
}

DI float gelu_tanh(float x) {
  float t = x * (-2.302208198f - 0.1029432397f * x * x);
  return x * __builtin_amdgcn_rcpf(1.0f + __builtin_amdgcn_exp2f(t));
}

DI void f1_worker(const Params& p, const Hs& hs, int fb, int rh, char* lds, const int wave) {
  const int lane = lane_id(), g = lane >> 4, r16 = lane & 15;
  const int wr = wave >> 1, wc = wave & 1;
  LAS char* L = (LAS char*)lds;
  const unsigned lbase = __builtin_amdgcn_readfirstlane((unsigned)(size_t)L);
  const rsrc_t rc = mkrsrc(p.ex + EX_FLAGS, F_END);
  unsigned budget = SPIN_BUDGET;
  auto weights = [&]() {
#pragma unroll 4
    for (int i = 0; i < 32; ++i) {
      int blk = wave * 32 + i;
      int row = blk * 4 + (lane >> 4);
      int ch = (lane & 15) ^ (((row & 3) << 2) | ((row >> 2) & 3));
      int f = fb * 64 + (ch & 7) * 8;
      if (f >= DF) f = 0;
      glds(((ch >> 3) ? p.wu : p.wg) + (size_t)row * DF + f, lbase + blk * 1024);
    }
  };
  if (fb < 16) starter(p, hs, fb * 2 + rh, wave, weights);
  else {
    arm_share(p, fb * 2 + rh - 32, hs, wave, budget);
    for (int i = 0; i < 256 && now32() - hs.lo < WT2; ++i) __builtin_amdgcn_s_sleep(4);
    weights();
  }
  WAITV(0);
  barrier_raw();
  bf16x8 wf[2][2][16];
  {
    const int q = r16 >> 2, pp = lane & 3;
#pragma unroll
    for (int up = 0; up < 2; ++up) {
      const unsigned o0 = off_b(8 * g + q, 8 * up + 4 * wc + pp), o1 = off_b(8 * g + 4 + q, 8 * up + 4 * wc + pp);
#pragma unroll
      for (int t = 0; t < 2; ++t)
#pragma unroll
        for (int kk = 0; kk < 16; ++kk) wf[up][t][kk] = tr_frag(L + o0 + kk * 8192 + 8 * t, L + o1 + kk * 8192 + 8 * t);
    }
  }
  f32x4 cs[2][2] = {};
  {
    const u32x4 one = {0x3f803f80u, 0x3f803f80u, 0x3f803f80u, 0x3f803f80u};
#pragma unroll
    for (int kk = 0; kk < 16; ++kk)
#pragma unroll
      for (int up = 0; up < 2; ++up)
#pragma unroll
        for (int t = 0; t < 2; ++t) cs[up][t] = MF(wf[up][t][kk], __builtin_bit_cast(bf16x8, one), cs[up][t]);
  }
  barrier_raw();
  const rsrc_t rhh = mkrsrc(p.ws + WS_H, 4 * MB);
  const rsrc_t ra = mkrsrc(p.ws + WS_ACT, S_ * DFP * 2);
  const bool pad = fb * 64 + wc * 32 >= DF;
  unsigned fw = 0xffffffffu;
  for (int s = rh; s < NSEG; s += 2) {
    if (wave == 0) {
      if (!__all(fw != 0xffffffffu)) fw = wait_flags_lazy(rc, (lane & 7) * 256, F_H + (unsigned)s * 4, budget, s < NSEG - NLAZY);
      if (s >= NSEG - NH2) wait_due(fw);
      if (fb < 16 && s < NSEG - GEND) {
        const unsigned tr = __builtin_amdgcn_raw_buffer_load_b32(rc, (unsigned)min(s + 2 + min(lane, FARW - 1), NSEG - 2) * 128 + 4, F_REC, 16);
        for (int i = 0; i < 64; ++i) {
          const unsigned d = now32() - tr + GATE0;
          if (!__any(tr != 0xffffffffu && d < (unsigned)(GATE0 + GATE1))) break;
          __builtin_amdgcn_s_sleep(1);
        }
      }
    }
    barrier_raw();
    {
      u32x4 hr[2][4];
      for (;;) {
        asm volatile("" ::: "memory");
#pragma unroll
        for (int it = 0; it < 2; ++it)
#pragma unroll
          for (int j = 0; j < 4; ++j) hr[it][j] = ld128(rhh, (unsigned)(((wave * 8 + it * 4 + g) * DM + (j * 16 + r16) * 8) * 2), (unsigned)s * (32 * DM * 2));
        SCHED();
        if (s < NSEG - NH2) break;
        unsigned m = 0;
#pragma unroll
        for (int j = 0; j < 8; ++j) m = max(m, umax4(hr[j >> 2][j & 3]));
        if (__all(m != 0xffffffffu) || budget == 0) break;
        --budget;
      }
      announce(rc, wave == 0 && s >= NSEG - NH2, F_ACT + (unsigned)s * 4 + fb * 256, FD);
#pragma unroll
      for (int it = 0; it < 2; ++it) {
        const int row = wave * 8 + it * 4 + g;
#pragma unroll
        for (int j = 0; j < 4; ++j) *(LAS u32x4*)(L + row * 1024 + (((j * 16 + r16) ^ (row & 15)) * 16)) = hr[it][j];
      }
#pragma unroll
      for (int it = 0; it < 2; ++it) {
        const bf16x2 one = __builtin_bit_cast(bf16x2, 0x3f803f80u);
        float sa[2] = {0.f, 0.f}, qa[2] = {0.f, 0.f};
#pragma unroll
        for (int j = 0; j < 16; ++j) {
          const unsigned u = hr[it][j >> 2][j & 3];
          const bf16x2 v = __builtin_bit_cast(bf16x2, u);
          sa[j & 1] = __builtin_amdgcn_fdot2_f32_bf16(v, one, sa[j & 1], false);
          qa[j & 1] = __builtin_amdgcn_fdot2_f32_bf16(v, v, qa[j & 1], false);
        }
        const float mean = rsum16(sa[0] + sa[1]) * (1.0f / DM);
        const float var = vmax(rsum16(qa[0] + qa[1]) * (1.0f / DM) - mean * mean, 0.f);
        const float rstd = __builtin_amdgcn_rsqf(var + 1e-5f);
        const u32x2 st = {__float_as_uint(rstd), __float_as_uint(-mean * rstd)};
        *(LAS u32x2*)(L + 32768 + (wave * 8 + it * 4 + g) * 8) = st;
      }
    }
    barrier_raw();
    fw = __builtin_amdgcn_raw_buffer_load_b32(rc, wave == 0 && s >= NSEG - NH2 ? (lane & 7) * 256 : OOB, F_H + (unsigned)min(s + 2, NSEG - 1) * 4, 16);
    f32x4 acc[2][2] = {};
    u32x2 st;
    {
      bf16x8 xf[16];
#pragma unroll
      for (int kk = 0; kk < 16; ++kk) xf[kk] = *(const LAS bf16x8*)(L + (wr * 16 + r16) * 1024 + (((4 * kk + g) ^ r16) * 16));
      st = *(const LAS u32x2*)(L + 32768 + (wr * 16 + r16) * 8);
      SCHED();
#pragma unroll
      for (int kk = 0; kk < 16; ++kk) {
#pragma unroll
        for (int up = 0; up < 2; ++up)
#pragma unroll
          for (int t = 0; t < 2; ++t) acc[up][t] = MF(wf[up][t][kk], xf[kk], acc[up][t]);
      }
    }
    {
      float o[8];
#pragma unroll
      for (int t = 0; t < 2; ++t)
#pragma unroll
        for (int r = 0; r < 4; ++r)
          o[t * 4 + r] = gelu_tanh(acc[0][t][r] * __uint_as_float(st[0]) + cs[0][t][r] * __uint_as_float(st[1])) * (acc[1][t][r] * __uint_as_float(st[0]) + cs[1][t][r] * __uint_as_float(st[1]));
      u32x4 v = {pack2(o[0], o[1]), pack2(o[2], o[3]), pack2(o[4], o[5]), pack2(o[6], o[7])};
      if (pad) { v[0] = 0u; v[1] = 0u; v[2] = 0u; v[3] = 0u; }
      st128(nosent4(v), ra, lane * 16, (unsigned)((s * 2 + wr) * 44 + fb * 2 + wc) * 1024);
    }
    if (s < NSEG - NH2) {
      fw = __builtin_amdgcn_raw_buffer_load_b32(rc, wave == 0 ? (lane & 7) * 256 : OOB, F_H + (unsigned)(s + 2) * 4, 16);
      WAITV(0);
    }
    barrier_raw();
    if (wave == 0 && s < NSEG - NH2) set_flag(rc, F_ACT + (unsigned)s * 4 + fb * 256);
  }
}

DI void f2_worker(const Params& p, const Hs& hs, int cb, int par2, int set, char* lds, const int wave) {
  const int lane = lane_id(), g = lane >> 4, r16 = lane & 15;
  LAS char* L = (LAS char*)lds;
  const unsigned lbase = __builtin_amdgcn_readfirstlane((unsigned)(size_t)L);
  const rsrc_t rc = mkrsrc(p.ex + EX_FLAGS, F_END);
  unsigned budget = SPIN_BUDGET;
  if (set == 0) {
    arm_share(p, 12 + cb * 2 + par2, hs, wave, budget);
    for (int i = 0; i < 256 && now32() - hs.lo < WT2; ++i) __builtin_amdgcn_s_sleep(4);
  }
  bf16x8 wf[4][11];
#pragma unroll
  for (int hf = 0; hf < 2; ++hf) {
#pragma unroll 2
    for (int i = 0; i < 22; ++i) {
      int blk = wave * 22 + i;
      int row = blk * 16 + (lane >> 2);
      int rs = row < DF ? row : 0;
      glds(p.wd + (size_t)rs * DM + cb * 64 + hf * 32 + (lane & 3) * 8, lbase + blk * 1024);
    }
    WAITV(0);
    const int q = r16 >> 2, pp = lane & 3;
#pragma unroll
    for (int t = 0; t < 2; ++t)
#pragma unroll
      for (int kk = 0; kk < 11; ++kk) {
        int row = 32 * (wave * 11 + kk) + 8 * g + q;
        wf[hf * 2 + t][kk] = tr_frag(L + row * 64 + t * 32 + pp * 8, L + (row + 4) * 64 + t * 32 + pp * 8);
      }
    LGKM0();
  }
  barrier_raw();
  const rsrc_t rh = mkrsrc(p.ws + WS_H, 4 * MB);
  const rsrc_t ra = mkrsrc(p.ws + WS_ACT, S_ * DFP * 2);
  const int orow = wave * 4 + g, oc = r16 * 4;
  bf16x8 xf[11];
  u32x2 hr;
  unsigned nx;
  auto next = [&](int s) { return s < F2S ? s + 1 : s + 3; };
  auto fetch = [&](int s) {
    asm volatile("" ::: "memory");
#pragma unroll
    for (int kk = 0; kk < 11; ++kk) xf[kk] = ldfrag(ra, lane * 16, (unsigned)((s * 2 + par2) * 44 + wave * 11 + kk) * 1024);
    hr = ld64(rh, (unsigned)((orow * DM + cb * 64 + oc) * 2), (unsigned)(s * 2 + par2) * (16 * DM * 2));
    nx = __builtin_amdgcn_raw_buffer_load_b32(rc, wave == 0 ? (lane < 22 ? lane : 0) * 256 : OOB, F_ACT + (unsigned)min(next(s), NSEG - 1) * 4, 16);
    SCHED();
  };
  bool known = false;
  for (int s = set ? F2S + set : 0; s < NSEG; s = next(s)) {
    if (!known) {
      if (wave == 0) {
        const unsigned fw = wait_flags_lazy(rc, (lane < 22 ? lane : 0) * 256, F_ACT + (unsigned)s * 4, budget, s < NSEG - NLAZY);
        if (s >= NSEG - NH2) wait_due(fw);
      }
      barrier_raw();
    }
    f32x4 acc[4];
    for (;;) {
      if (!known) fetch(s);
      known = false;
      unsigned mm = 0;
#pragma unroll
      for (int t = 0; t < 4; ++t) acc[t] = f32x4{0.f, 0.f, 0.f, 0.f};
#pragma unroll
      for (int kk = 0; kk < 11; ++kk) {
#pragma unroll
        for (int t = 0; t < 4; ++t) acc[t] = MF(wf[t][kk], xf[kk], acc[t]);
        mm = max(mm, umax4f(xf[kk]));
      }
      mm = max(mm, max(hr[0], hr[1]));
      if (s < NSEG - NH2 || __all(mm != 0xffffffffu) || budget == 0) break;
      --budget;
    }
    LAS char* B = L + (s & 1) * 32768;
#pragma unroll
    for (int t = 0; t < 4; ++t) *(LAS f32x4*)(B + wave * 4352 + r16 * 272 + (t * 16 + g * 4) * 4) = acc[t];
    if (wave == 0) *(LAS unsigned*)(B + 17408) = __all(nx == 1u) && next(s) < NSEG;
    f32x4 h0 = {__uint_as_float(hr[0] << 16), __uint_as_float(hr[0] & 0xffff0000u), __uint_as_float(hr[1] << 16), __uint_as_float(hr[1] & 0xffff0000u)};
    barrier_raw();
    known = __builtin_amdgcn_readfirstlane(*(const LAS unsigned*)(B + 17408)) != 0;
    f32x4 u[4];
#pragma unroll
    for (int w = 0; w < 4; ++w) u[w] = *(const LAS f32x4*)(B + w * 4352 + orow * 272 + oc * 4);
    if (known) fetch(next(s));
    h0 += u[0]; h0 += u[1]; h0 += u[2]; h0 += u[3];
    u32x2 v = {pack2(h0[0], h0[1]), pack2(h0[2], h0[3])};
    __builtin_amdgcn_raw_buffer_store_b64(v, mkrsrc(p.out, S_ * DM * 2), (unsigned)((orow * DM + cb * 64 + oc) * 2), (unsigned)(s * 2 + par2) * (16 * DM * 2), OAUX);
  }
}

__global__ __launch_bounds__(256, 1) void k_mega(Params p) {
  extern __shared__ __attribute__((aligned(16))) char lds[];
  const int b = blockIdx.x;
  const int x = b & 7, c = b >> 3;
  const int wave = __builtin_amdgcn_readfirstlane(threadIdx.x >> 6);
  if (b > 252) return;
  unsigned kw, kx;
  asm volatile("s_load_dword %0, %2, 0x0\n\ts_load_dword %1, %2, 0x40" : "=&s"(kw), "=&s"(kx) : "s"(__builtin_amdgcn_kernarg_segment_ptr()));
  Hs hs;
  {
    const unsigned long long n = __builtin_amdgcn_s_memrealtime();
    hs.lo = (unsigned)n; hs.hi = (unsigned)(n >> 32); hs.blk = x * 32 + c;
    volatile LAS unsigned* nw = (volatile LAS unsigned*)((LAS char*)lds + L_TOTAL - 8);
    if (wave == 0) { nw[0] = hs.lo; nw[1] = hs.hi; }
    barrier_raw();
    asm volatile("" :: "s"(kw), "s"(kx));
    hs.lo = __builtin_amdgcn_readfirstlane(nw[0]); hs.hi = __builtin_amdgcn_readfirstlane(nw[1]);
    barrier_raw();
  }
#define CLEAN() __builtin_amdgcn_s_waitcnt(0)
#define ASK() do { CLEAN(); if (wave == 0) hs_post(mkrsrc(args_again().ex + EX_FLAGS, F_END), hs, 0u); } while (0)
  if (x < 4) {
    const int j = (x >> 1) * 64 + c * 2 + (x & 1);
    if (j < 32) { ASK(); a_head(args_again(), hs, j, lds, wave); }
    else if (j < 64) { ASK(); v_head(args_again(), hs, j - 32, lds, wave); }
    else if (j < 96) { ASK(); reducer(j - 64, lds, wave); }
    else { ASK(); f1_worker(args_again(), hs, (j - 96) >> 1, j & 1, lds, wave); }
  } else {
    const int y = x - 4, d = c;
    if (d < 16) {
      ASK(); vw_worker(args_again(), hs, (d & 7) * 4 + y, d >> 3, lds, wave);
      if (d >= 8) { CLEAN(); out_worker(args_again(), d - 8, y, lds, wave); }
    }
    else if (d >= 24 && d < 27) { ASK(); f1_worker(args_again(), hs, 16 + (d - 24) + 3 * (y & 1), y >> 1, lds, wave); }
    else if (d < 31) {
      ASK();
      if (d < 24) { qk_worker(args_again(), hs, (d - 16) * 4 + y, lds, wave); CLEAN(); }
      f2_worker(args_again(), hs, (d & 3) + 4 * (y & 1), y >> 1, d < 24 ? (d >> 2) - 3 : 0, lds, wave);
    }
    else { ASK(); unsigned budget = SPIN_BUDGET; ln_rest(args_again(), hs, 0, wave, budget); }
  }
}

extern "C" void kernel_launch(void* const* d_in, const int* in_sizes, int n_in, void* d_out, int out_size, void* d_ws,
                              size_t ws_size, hipStream_t stream) {
  Params p;
  p.x = (const u16*)d_in[0]; p.mem0 = (const u16*)d_in[1]; p.wq = (const u16*)d_in[2]; p.wk = (const u16*)d_in[3];
  p.wv = (const u16*)d_in[4]; p.wo = (const u16*)d_in[5];
  p.wg = (const u16*)d_in[8]; p.wu = (const u16*)d_in[9]; p.wd = (const u16*)d_in[10];
  p.out = (u16*)d_out; p.ws = (char*)d_ws;
  uintptr_t exa = ((uintptr_t)d_ws + WS_EX_MIN + ((2u << 20) - 1)) & ~(uintptr_t)((2u << 20) - 1);
  p.ex = (char*)exa;
  (void)hipFuncSetAttribute((const void*)k_mega, hipFuncAttributeMaxDynamicSharedMemorySize, L_TOTAL);
  k_mega<<<256, 256, L_TOTAL, stream>>>(p);
}
```
